```python
import torch

def _softmax(x, axis=-1):
    return torch.softmax(x, dim=axis)
def _gelu(x):
    return torch.nn.functional.gelu(x, approximate="tanh")
def _silu(x):
    return torch.nn.functional.silu(x)
def _sigmoid(x):
    return torch.sigmoid(x)
def _layernorm(x, axis=-1, eps=1e-5):
    m = x.mean(dim=axis, keepdim=True)
    v = x.var(dim=axis, keepdim=True, unbiased=False)
    return (x - m) / torch.sqrt(v + eps)
def _scatter_add(tgt, idx, src, axis=0):
    return tgt.clone().index_add_(axis, idx, src)
def _segment_sum(src, seg, n):
    out = torch.zeros((n,) + src.shape[1:], dtype=src.dtype)
    return out.index_add_(0, seg, src)
def _pad(x, pw):
    flat = tuple(v for pair in reversed(pw) for v in pair)
    return torch.nn.functional.pad(x, flat)

kernel_name = 'recurrent_scan__gqa_block_cached__geglu_ffn__decode'

def setup_inputs():

    _w = (lambda a: (lambda t: t.to(torch.bfloat16) if t.is_floating_point() else t)(torch.as_tensor(a)))
    def _A_setup():
        B, Sq, D = 1, 1, 576
        Hq, Hkv, Sk, Dh = 32, 16, 65536, 128
        assert Hq % Hkv == 0, (Hq, Hkv)
        return {
            "x": _w((lambda *s: torch.randn(*s, dtype=torch.bfloat16) * 0.05)(B, Sq, D)),
            "k_cache": _w((lambda *s: torch.randn(*s, dtype=torch.bfloat16) * 1.6)(B, Hkv, Sk, Dh)),
            "v_cache": _w((lambda *s: torch.randn(*s, dtype=torch.bfloat16) * 0.2)(B, Hkv, Sk, Dh)),
            "wq": _w((lambda *s: torch.randn(*s, dtype=torch.bfloat16) * 1.6)(D, Hq * Dh) * (D ** -0.5)),
            "wo": _w((lambda *s: torch.randn(*s, dtype=torch.bfloat16) * 0.2)(Hq * Dh, D) * ((Hq * Dh) ** -0.5)),
        }
    _A_in = _A_setup()

    def _B_setup():
        B, S, D, F = 1, 1, 576, 4608
        return {
            "x": _w((lambda *s: torch.randn(*s, dtype=torch.bfloat16) * 0.05)(B, S, D)),
            "w_gate": _w((lambda *s: torch.randn(*s, dtype=torch.bfloat16) * 0.1)(D, F)),
            "w_up": _w((lambda *s: torch.randn(*s, dtype=torch.bfloat16) * 0.1)(D, F)),
            "w_down": _w((lambda *s: torch.randn(*s, dtype=torch.bfloat16) * 0.1)(F, D)),
        }
    _B_in = _B_setup()

    return {
        "A_x": _A_in["x"],
        "A_k_cache": _A_in["k_cache"],
        "A_v_cache": _A_in["v_cache"],
        "A_wq": _A_in["wq"],
        "A_wo": _A_in["wo"],
        "B_x": _B_in["x"],
        "B_w_gate": _B_in["w_gate"],
        "B_w_up": _B_in["w_up"],
        "B_w_down": _B_in["w_down"],
    }

def reference(A_x, A_k_cache, A_v_cache, A_wq, A_wo, B_x, B_w_gate, B_w_up, B_w_down):
    def _A_ref(x, k_cache, v_cache, wq, wo):
        B, Sq, D = x.shape
        Hq, Dh = 32, 128
        Hkv = k_cache.shape[1]
        rep = Hq // Hkv
        q = (lambda x, *d: x.permute(*d))((x @ wq).reshape(B, Sq, Hq, Dh), 0, 2, 1, 3)
        k = (lambda x, n, axis: torch.repeat_interleave(x, n, dim=axis))(k_cache, rep, axis=1)
        v = (lambda x, n, axis: torch.repeat_interleave(x, n, dim=axis))(v_cache, rep, axis=1)
        scores = torch.einsum("bhqd,bhkd->bhqk", q, k) * (Dh ** -0.5)
        p = _softmax(scores, axis=-1)
        o = torch.einsum("bhqk,bhkd->bhqd", p, v)
        return (lambda x, *d: x.permute(*d))(o, 0, 2, 1, 3).reshape(B, Sq, Hq * Dh) @ wo

    def _B_ref(x, w_gate, w_up, w_down):
        return (_gelu(x @ w_gate) * (x @ w_up)) @ w_down

    x = A_x
    xn = _layernorm(x, axis=-1)
    s = _A_ref(xn, A_k_cache, A_v_cache, A_wq, A_wo)
    g = _silu(_B_ref(xn, B_w_gate, B_w_up, B_w_down))
    return x + s * g
```

```cpp
#include <hip/hip_runtime.h>
#include <hip/hip_bf16.h>
#include <stdint.h>
#define MIMIC 0
#define REF_NS refk
namespace REF_NS {
typedef unsigned short u16;
__device__ __forceinline__ float b2f(u16 v) { return __uint_as_float((unsigned)v << 16); }
__device__ __forceinline__ u16 f2b(float f) { unsigned u = __float_as_uint(f); u += 0x7fffu + ((u >> 16) & 1u); return (u16)(u >> 16); }
__device__ __forceinline__ float rb(float f) { return MIMIC ? b2f(f2b(f)) : f; }
constexpr int D = 576, HQ = 32, HKV = 16, SK = 65536, DH = 128, F = 4608, NS = 64;

__device__ void layernorm(const u16* x, float* xn  ) {
  float s = 0.f; for (int i = 0; i < D; ++i) s += b2f(x[i]);
  const float mean = s / D; float v = 0.f;
  for (int i = 0; i < D; ++i) { const float d = b2f(x[i]) - mean; v += d * d; }
  v /= D;
  const float m = rb(mean), vv = rb(v), den = rb(sqrtf(rb(vv + 1e-5f)));
  for (int i = threadIdx.x; i < D; i += blockDim.x) xn[i] = rb(rb(b2f(x[i]) - m) / den);
  __syncthreads();
}
__global__ void k_proj(const u16* x, const u16* wq, const u16* wg, const u16* wu, float* q, float* h) {
  __shared__ float xn[D];
  layernorm(x, xn);
  const int c = blockIdx.x * blockDim.x + threadIdx.x;
  if (c < HQ * DH) { float a = 0.f; for (int i = 0; i < D; ++i) a += xn[i] * b2f(wq[(size_t)i * HQ * DH + c]); q[c] = rb(a); }
  else if (c < HQ * DH + F) { const int j = c - HQ * DH; float a = 0.f, b = 0.f;
    for (int i = 0; i < D; ++i) { a += xn[i] * b2f(wg[(size_t)i * F + j]); b += xn[i] * b2f(wu[(size_t)i * F + j]); }
    a = rb(a); b = rb(b);
    const float ge = rb(0.5f * a * (1.f + tanhf(0.7978845608028654f * (a + 0.044715f * a * a * a))));
    h[j] = rb(ge * b); }
}
__global__ void k_scores(const float* q, const u16* k, float* sc) {
  const int hq = blockIdx.y, key = blockIdx.x * blockDim.x + threadIdx.x;
  const u16* kr = k + ((size_t)(hq / 2) * SK + key) * DH; const float* qh = q + hq * DH;
  float a = 0.f; for (int d = 0; d < DH; ++d) a += qh[d] * b2f(kr[d]);
  sc[(size_t)hq * SK + key] = rb(rb(a) * 0.08838834764831845f);
}
__global__ void k_softmax(float* sc) {
  __shared__ float red[1024];
  float* s = sc + (size_t)blockIdx.x * SK;
  float m = -INFINITY; for (int i = threadIdx.x; i < SK; i += 1024) m = fmaxf(m, s[i]);
  red[threadIdx.x] = m; __syncthreads();
  for (int o = 512; o; o >>= 1) { if (threadIdx.x < o) red[threadIdx.x] = fmaxf(red[threadIdx.x], red[threadIdx.x + o]); __syncthreads(); }
  m = red[0]; __syncthreads();
  float l = 0.f; for (int i = threadIdx.x; i < SK; i += 1024) l += expf(s[i] - m);
  red[threadIdx.x] = l; __syncthreads();
  for (int o = 512; o; o >>= 1) { if (threadIdx.x < o) red[threadIdx.x] += red[threadIdx.x + o]; __syncthreads(); }
  l = red[0];
  for (int i = threadIdx.x; i < SK; i += 1024) s[i] = rb(expf(s[i] - m) / l);
}
__global__ void k_pv(const float* p, const u16* v, float* part) {
  const int hq = blockIdx.y, sp = blockIdx.x, d = threadIdx.x;
  float a = 0.f;
  for (int key = sp * (SK / NS); key < (sp + 1) * (SK / NS); ++key) a += p[(size_t)hq * SK + key] * b2f(v[((size_t)(hq / 2) * SK + key) * DH + d]);
  part[((size_t)hq * NS + sp) * DH + d] = a;
}
__global__ void k_o(const float* part, float* o) { const int hq = blockIdx.x, d = threadIdx.x; float a = 0.f; for (int s = 0; s < NS; ++s) a += part[((size_t)hq * NS + s) * DH + d]; o[hq * DH + d] = rb(a); }
__global__ void k_final(const u16* x, const float* o, const float* h, const u16* wo, const u16* wd, u16* out, float* outf) {
  __shared__ float rs[256], rf[256];
  const int c = blockIdx.x;
  float s = 0.f, f = 0.f;
  for (int i = threadIdx.x; i < HQ * DH; i += 256) s += o[i] * b2f(wo[(size_t)i * D + c]);
  for (int i = threadIdx.x; i < F; i += 256) f += h[i] * b2f(wd[(size_t)i * D + c]);
  rs[threadIdx.x] = s; rf[threadIdx.x] = f; __syncthreads();
  for (int t = 128; t; t >>= 1) { if (threadIdx.x < t) { rs[threadIdx.x] += rs[threadIdx.x + t]; rf[threadIdx.x] += rf[threadIdx.x + t]; } __syncthreads(); }
  if (threadIdx.x == 0) { s = rb(rs[0]); f = rb(rf[0]); const float g = rb(f / (1.f + expf(-f))); const float r = b2f(x[c]) + rb(s * g); out[c] = f2b(r); if (outf) { outf[c] = r; outf[D + c] = s; outf[2 * D + c] = g; } }
}
static void launch(void* const* d_in, void* d_out, void* d_ws, hipStream_t stream, float* outf) {
  const u16 *x = (const u16*)d_in[0], *k = (const u16*)d_in[1], *v = (const u16*)d_in[2], *wq = (const u16*)d_in[3], *wo = (const u16*)d_in[4];
  const u16 *wg = (const u16*)d_in[6], *wu = (const u16*)d_in[7], *wd = (const u16*)d_in[8];
  float* ws = (float*)d_ws; float *q = ws, *h = ws + 4096, *o = ws + 16384, *part = ws + 32768, *sc = ws + (1 << 20);
  k_proj<<<(HQ * DH + F) / 64, 64, 0, stream>>>(x, wq, wg, wu, q, h);
  k_scores<<<dim3(SK / 256, HQ), 256, 0, stream>>>(q, k, sc);
  k_softmax<<<HQ, 1024, 0, stream>>>(sc);
  k_pv<<<dim3(NS, HQ), 128, 0, stream>>>(sc, v, part);
  k_o<<<HQ, 128, 0, stream>>>(part, o);
  k_final<<<D, 256, 0, stream>>>(x, o, h, wo, wd, (u16*)d_out, outf);
}
}
extern "C" void kernel_launch(void* const* d_in, const int* in_sizes, int n_in, void* d_out, int out_size, void* d_ws, size_t ws_size, hipStream_t stream) {
  REF_NS::launch(d_in, d_out, d_ws, stream, nullptr);
}
```

```cpp
#include <hip/hip_runtime.h>
#include <hip/hip_bf16.h>
#include <stdint.h>

#define LAS __attribute__((address_space(3)))
#define GAS __attribute__((address_space(1)))
typedef unsigned u32;
typedef unsigned long long u64;
typedef unsigned short u16;
typedef unsigned u32x4 __attribute__((ext_vector_type(4)));
typedef unsigned u32x2 __attribute__((ext_vector_type(2)));
typedef float f32x4 __attribute__((ext_vector_type(4)));
typedef float f32x2 __attribute__((ext_vector_type(2)));
typedef __bf16 bf16x2 __attribute__((ext_vector_type(2)));
typedef __bf16 bf16x8 __attribute__((ext_vector_type(8)));
typedef short s16x4 __attribute__((ext_vector_type(4)));

#define P_WN 20
#define P_NSLOT 8
#define P_NSTAT 120
#define P_AHEAD 3

namespace fk {
constexpr int DM = 576, NHKV = 16, SK = 65536, DH = 128, FF = 4608, NQ = 4096;
constexpr int NWG = 256, NTHR = 512;
constexpr int WN = P_WN, NSLOT = P_NSLOT, SLOTB = 16384, NCONS = 4, NSCH = 3;
constexpr int NT = SK / 32;
constexpr int NSTAT = P_NSTAT;
constexpr int DYN0 = NSTAT * 16;
constexpr int AHEAD = P_AHEAD;
static_assert(NSTAT <= NT / 16 && NSTAT >= NSLOT && AHEAD <= 7 && WN > 16 && WN <= 60, "knobs");
constexpr u32 WS_E = 0, WS_CNT = 4096, WS_Q = 16384, WS_H = 32768, WS_P = 65536;
constexpr u32 P_STRIDE = 144 * 8;
constexpr u32 WS_Y = WS_P + 32 * 16 * P_STRIDE, WS_F = WS_Y + 64 * DM * 8, WS_END = WS_F + 64 * DM * 8;
constexpr u32 L_CTL = NSLOT * SLOTB;
constexpr u32 L_QS = L_CTL + 256;
constexpr u32 L_RS = L_QS + 512;
constexpr u32 L_HS = L_RS + 1024;
constexpr u32 L_RED = L_HS + 1024;
constexpr u32 L_MRG = L_RED + 8 * 144 * 4;
constexpr u32 L_OS = L_MRG + 4 * 2 * 132 * 4;
constexpr u32 L_FIN = L_OS + 64 * 8 * 4;
constexpr u32 L_END = L_FIN + 8 * 8 * 4 * 4;
static_assert(L_END <= 163840 && L_END > 81920, "LDS");
enum { C_PUB = 0, C_ISS = 1, C_QRDY = 2, C_TAG = 3, C_CNTQ = 4, C_CNTF0 = 5, C_CNTF1 = 6, C_CNTD = 7, C_TK = 8, C_FREE = 16 };
constexpr u32 SPIN_LDS = 1u << 22, SPIN_GLB = 1u << 19;

struct Params {
  const u16 *x, *k, *v, *wq, *wo, *wg, *wu, *wd;
  u16* out;
  unsigned char* ws;
};

__device__ __forceinline__ float bflo(u32 u) { return __uint_as_float(u << 16); }
__device__ __forceinline__ float bfhi(u32 u) { return __uint_as_float(u & 0xffff0000u); }
__device__ __forceinline__ u32 pk2(float lo, float hi) { f32x2 f = {lo, hi}; return __builtin_bit_cast(u32, __builtin_convertvector(f, bf16x2)); }
__device__ __forceinline__ float rb(float f) { return bflo(pk2(f, 0.f) & 0xffffu); }
template <int CTRL> __device__ __forceinline__ float dpp(float x) {
  return __builtin_bit_cast(float, __builtin_amdgcn_mov_dpp(__builtin_bit_cast(int, x), CTRL, 0xf, 0xf, true));
}
constexpr int XOR1 = 0xB1, XOR2 = 0x4E, ROR4 = 0x124, ROR8 = 0x128;
__device__ __forceinline__ float x16_sum(float x) { auto s = __builtin_amdgcn_permlane16_swap(__float_as_uint(x), __float_as_uint(x), false, false); return __uint_as_float(s[0]) + __uint_as_float(s[1]); }
__device__ __forceinline__ float x32_sum(float x) { auto s = __builtin_amdgcn_permlane32_swap(__float_as_uint(x), __float_as_uint(x), false, false); return __uint_as_float(s[0]) + __uint_as_float(s[1]); }
__device__ __forceinline__ float x16_max(float x) { auto s = __builtin_amdgcn_permlane16_swap(__float_as_uint(x), __float_as_uint(x), false, false); return fmaxf(__uint_as_float(s[0]), __uint_as_float(s[1])); }
__device__ __forceinline__ float x32_max(float x) { auto s = __builtin_amdgcn_permlane32_swap(__float_as_uint(x), __float_as_uint(x), false, false); return fmaxf(__uint_as_float(s[0]), __uint_as_float(s[1])); }
__device__ __forceinline__ float row16_sum(float x) { x += dpp<XOR1>(x); x += dpp<XOR2>(x); x += dpp<ROR4>(x); x += dpp<ROR8>(x); return x; }
__device__ __forceinline__ float row16_max(float x) { x = fmaxf(x, dpp<XOR1>(x)); x = fmaxf(x, dpp<XOR2>(x)); x = fmaxf(x, dpp<ROR4>(x)); x = fmaxf(x, dpp<ROR8>(x)); return x; }
__device__ __forceinline__ float wave_sum(float x) { return x32_sum(x16_sum(row16_sum(x))); }
__device__ __forceinline__ void lds_wait() { asm volatile("s_waitcnt lgkmcnt(0)" ::: "memory"); }
__device__ __forceinline__ void vm_drain() { asm volatile("s_waitcnt vmcnt(0)" ::: "memory"); }
__device__ __forceinline__ void wg_barrier() { asm volatile("s_waitcnt lgkmcnt(0)\n\ts_barrier" ::: "memory"); }
__device__ __forceinline__ u32x4 ldnt(const u16* p) { return __builtin_nontemporal_load((const GAS u32x4*)p); }
__device__ __forceinline__ u32x4 mk_srd(const void* p, u32 bytes) {
  const u64 a = (u64)p;
  u32x4 r; r.x = __builtin_amdgcn_readfirstlane((u32)a); r.y = __builtin_amdgcn_readfirstlane((u32)(a >> 32) & 0xffffu); r.z = bytes; r.w = 0x00020000u; return r;
}
__device__ __forceinline__ u32x4 g_ld(__amdgpu_buffer_rsrc_t r, u32 off) { asm volatile("" ::: "memory"); return __builtin_amdgcn_raw_buffer_load_b128(r, off, 0, 16); }
__device__ __forceinline__ void g_st(__amdgpu_buffer_rsrc_t r, u32 off, u32 a, u32 b, u32 tag) { u32x4 v = {a, tag, b, tag}; __builtin_amdgcn_raw_buffer_store_b128(v, r, off, 0, 16); }

__device__ __forceinline__ void dma4(u32 ldsbase, u32 v0, u32 v1, u32 v2, u32 v3, u32x4 srd, u32 soff) {
  asm volatile("s_nop 4\n\ts_mov_b32 m0, %0\n\t"
               "s_waitcnt vmcnt(%c7)\n\t"
               "buffer_load_dwordx4 %1, %5, %6 offen nt lds\n\t"
               "s_waitcnt vmcnt(%c7)\n\t"
               "buffer_load_dwordx4 %2, %5, %6 offen offset:1024 nt lds\n\t"
               "s_waitcnt vmcnt(%c7)\n\t"
               "buffer_load_dwordx4 %3, %5, %6 offen offset:2048 nt lds\n\t"
               "s_waitcnt vmcnt(%c7)\n\t"
               "buffer_load_dwordx4 %4, %5, %6 offen offset:3072 nt lds"
               :: "s"(ldsbase), "v"(v0), "v"(v1), "v"(v2), "v"(v3), "s"(srd), "s"(soff), "i"(WN - 1) : "memory");
}

__device__ __forceinline__ void fma8(float (&acc)[8], u32x4 w, float xv) {
#pragma unroll
  for (int k = 0; k < 4; ++k) { acc[2 * k] = fmaf(xv, bflo(w[k]), acc[2 * k]); acc[2 * k + 1] = fmaf(xv, bfhi(w[k]), acc[2 * k + 1]); }
}
__device__ __forceinline__ float par_sum(float x) { x += dpp<XOR2>(x); x += dpp<ROR4>(x); x += dpp<ROR8>(x); return x32_sum(x16_sum(x)); }

__global__ void __launch_bounds__(NTHR, 2) fused_decode(Params p) {
  __shared__ __attribute__((aligned(16))) unsigned char lds[L_END];
  LAS unsigned char* L8 = (LAS unsigned char*)lds;
  volatile LAS u32* CTL = (volatile LAS u32*)(L8 + L_CTL);
  const int b = blockIdx.x, tid = threadIdx.x, lane = tid & 63;
  const int wave = __builtin_amdgcn_readfirstlane(tid >> 6);
  const int g = b >> 4, wi = b & 15;
  const int rg = (b & 7) | ((b >> 5) << 3), cg = (b >> 3) & 3;
  const __amdgpu_buffer_rsrc_t rws = __builtin_amdgcn_make_buffer_rsrc(p.ws, 0, WS_END, 0x00020000);
  u32 tag = 0;
  u32x4 wot[8];

  if (wave == 0) {
    __builtin_amdgcn_s_setprio(3);
    const u32x4 rk = mk_srd(p.k + (size_t)g * SK * DH, SK * DH * 2), rv = mk_srd(p.v + (size_t)g * SK * DH, SK * DH * 2);
    const u32 r4 = lane >> 4, pc = lane & 15;
    const u32 vk0 = r4 * 256 + ((pc ^ r4) << 4), vk1 = r4 * 256 + ((pc ^ (4 + r4)) << 4), vk2 = r4 * 256 + ((pc ^ (8 + r4)) << 4), vk3 = r4 * 256 + ((pc ^ (12 + r4)) << 4);
    const u32 vv0 = r4 * 256 + ((pc ^ (r4 << 1)) << 4), vv1 = r4 * 256 + ((pc ^ ((4 + r4) << 1)) << 4);
    const u32 ldsb = (u32)(u64)L8;
    int q = 0, pub = 0, x = NSTAT, dead = 0, s = 0;
    for (;; ++q) {
      int pos;
      if (q < NSTAT) pos = q * 16 + wi;
      else {
        bool fin = false;
        for (;;) {
          const int sc = (x - NSTAT) % NSCH;
          if (!((dead >> sc) & 1)) {
            u32 e = __builtin_amdgcn_readfirstlane(CTL[C_TK + (x & 7)]);
            if ((int)(e >> 16) != x + 1) {
              vm_drain(); pub = q; CTL[C_PUB] = pub;
              u32 sp = 0;
              while ((int)((e = __builtin_amdgcn_readfirstlane(CTL[C_TK + (x & 7)])) >> 16) != x + 1 && ++sp < SPIN_LDS) __builtin_amdgcn_s_sleep(1);
              if (sp >= SPIN_LDS) { fin = true; break; }
            }
            pos = DYN0 + (int)(e & 0xffffu);
            if (pos < NT) { ++x; break; }
            dead |= 1 << sc;
            if (dead == (1 << NSCH) - 1) { fin = true; break; }
          }
          ++x; CTL[C_ISS] = x;
        }
        if (fin) break;
      }
      if (q == 1) wg_barrier();
      if (q >= 1) CTL[C_ISS] = q < NSTAT ? q + 1 : x;
      if (q >= NSLOT) {
        const u32 need = q - NSLOT + 1;
        if (CTL[C_FREE + s] < need) {
          vm_drain(); pub = q; CTL[C_PUB] = pub;
          u32 sp = 0; while (CTL[C_FREE + s] < need && ++sp < SPIN_LDS) __builtin_amdgcn_s_sleep(1);
        }
      }
      const u32 so = __builtin_amdgcn_readfirstlane((u32)pos * 8192u);
      const u32 lb = __builtin_amdgcn_readfirstlane(ldsb + s * SLOTB);
#pragma unroll
      for (int gr = 0; gr < 4; ++gr) {
        if (gr < 2) dma4(lb + gr * 4096, vk0, vk1, vk2, vk3, rk, so + gr * 4096);
        else dma4(lb + gr * 4096, vv0, vv1, vv0, vv1, rv, so + (gr - 2) * 4096);
        if (((gr * 4 + 4 - WN) % 16 + 16) % 16 < 4) {
          const int done = q * 16 + gr * 4 + 4 - WN;
          const int c = done >= 0 ? done >> 4 : 0;
          if (c > pub) { pub = c; CTL[C_PUB] = pub; }
        }
      }
      s = s + 1 == NSLOT ? 0 : s + 1;
    }
    asm volatile("s_waitcnt vmcnt(16)" ::: "memory");
    if (q - 1 > pub) CTL[C_PUB] = q - 1;
    vm_drain();
    CTL[C_PUB] = (u32)q | 0x80000000u;
    CTL[C_ISS] = 0x40000000u;
    __builtin_amdgcn_s_setprio(0);
  } else if (wave <= 3) {
    const int hw = wave - 1, tau = tid - 64;
    const int half = tau & 1, rho = tau >> 1;
    const int xcd8 = b & 7, idx = b >> 3;
    const int cq = xcd8 * 32 + idx;
    const int cf = xcd8 * 36 + idx;
    const bool two = idx < 4;
    const u32 E = __hip_atomic_load((GAS u32*)(p.ws + WS_E), __ATOMIC_RELAXED, __HIP_MEMORY_SCOPE_AGENT);
    if (hw == 0 && (cq & 15) == 0 && lane == 0) __hip_atomic_store((GAS u32*)(p.ws + WS_CNT + (cq >> 4) * 256), 0u, __ATOMIC_RELAXED, __HIP_MEMORY_SCOPE_AGENT);
    asm volatile("" ::: "memory");
    u16 xs[9], xm[6];
#pragma unroll
    for (int i = 0; i < 9; ++i) xs[i] = p.x[lane + 64 * i];
#pragma unroll
    for (int i = 0; i < 6; ++i) xm[i] = p.x[rho + 96 * i];
    u32x4 wq_[6], wg_[6], wu_[6];
#pragma unroll
    for (int i = 0; i < 6; ++i) wq_[i] = ldnt(p.wq + (size_t)(rho + 96 * i) * NQ + cq * 16 + half * 8);
#pragma unroll
    for (int i = 0; i < 6; ++i) { wg_[i] = ldnt(p.wg + (size_t)(rho + 96 * i) * FF + cf * 16 + half * 8); wu_[i] = ldnt(p.wu + (size_t)(rho + 96 * i) * FF + cf * 16 + half * 8); }
    if (hw == 0) CTL[lane] = 0;
    __builtin_amdgcn_sched_barrier(0);
    wg_barrier();
    __builtin_amdgcn_sched_barrier(0);
    tag = __builtin_amdgcn_readfirstlane(E) + 1u;
    if (hw == 0 && lane == 0) CTL[C_TAG] = tag;
    float sum = 0.f;
#pragma unroll
    for (int i = 0; i < 9; ++i) sum += bflo(xs[i]);
    const float mean = wave_sum(sum) * (1.f / DM);
    float var = 0.f;
#pragma unroll
    for (int i = 0; i < 9; ++i) { const float d = bflo(xs[i]) - mean; var = fmaf(d, d, var); }
    var = wave_sum(var) * (1.f / DM);
    const float mb = rb(mean), den = rb(sqrtf(rb(rb(var) + 1e-5f)));
    float xn[6];
#pragma unroll
    for (int i = 0; i < 6; ++i) xn[i] = rb(rb(bflo(xm[i]) - mb) / den);
    LAS float* RS = (LAS float*)(L8 + L_RS);
    {
      float a[8] = {0.f, 0.f, 0.f, 0.f, 0.f, 0.f, 0.f, 0.f};
#pragma unroll
      for (int i = 0; i < 6; ++i) fma8(a, wq_[i], xn[i]);
#pragma unroll
      for (int k = 0; k < 8; ++k) a[k] = par_sum(a[k]);
      if (lane < 2) { *(LAS f32x4*)(RS + hw * 16 + lane * 8) = f32x4{a[0], a[1], a[2], a[3]}; *(LAS f32x4*)(RS + hw * 16 + lane * 8 + 4) = f32x4{a[4], a[5], a[6], a[7]}; }
      if (hw == 0) asm volatile("s_waitcnt vmcnt(12)" ::: "memory");
      lds_wait();
      u32 old = 0; if (lane == 0) old = __hip_atomic_fetch_add((LAS u32*)&CTL[C_CNTQ], 1u, __ATOMIC_RELAXED, __HIP_MEMORY_SCOPE_WORKGROUP);
      old = __builtin_amdgcn_readfirstlane(old);
      if (old == 2) {
        asm volatile("" ::: "memory");
        if (lane < 4) {
          const f32x4 t = *(LAS f32x4*)(RS + lane * 4) + *(LAS f32x4*)(RS + 16 + lane * 4) + *(LAS f32x4*)(RS + 32 + lane * 4);
          g_st(rws, WS_Q + (u32)(cq * 8 + lane * 2) * 8, pk2(t[0], t[1]), pk2(t[2], t[3]), tag);
        }
      }
    }
#pragma unroll 1
    for (int pass = 0; pass < (two ? 2 : 1); ++pass) {
      const int cs = pass == 0 ? cf : xcd8 * 36 + 32 + idx;
      if (pass == 1) {
#pragma unroll
        for (int i = 0; i < 6; ++i) { wg_[i] = ldnt(p.wg + (size_t)(rho + 96 * i) * FF + cs * 16 + half * 8); wu_[i] = ldnt(p.wu + (size_t)(rho + 96 * i) * FF + cs * 16 + half * 8); }
      }
      float a[8] = {0.f, 0.f, 0.f, 0.f, 0.f, 0.f, 0.f, 0.f}, c[8] = {0.f, 0.f, 0.f, 0.f, 0.f, 0.f, 0.f, 0.f};
#pragma unroll
      for (int i = 0; i < 6; ++i) { fma8(a, wg_[i], xn[i]); fma8(c, wu_[i], xn[i]); }
#pragma unroll
      for (int k = 0; k < 8; ++k) { a[k] = par_sum(a[k]); c[k] = par_sum(c[k]); }
      LAS float* RG = RS + 48 + pass * 96; LAS float* RU = RG + 48;
      if (lane < 2) {
        *(LAS f32x4*)(RG + hw * 16 + lane * 8) = f32x4{a[0], a[1], a[2], a[3]}; *(LAS f32x4*)(RG + hw * 16 + lane * 8 + 4) = f32x4{a[4], a[5], a[6], a[7]};
        *(LAS f32x4*)(RU + hw * 16 + lane * 8) = f32x4{c[0], c[1], c[2], c[3]}; *(LAS f32x4*)(RU + hw * 16 + lane * 8 + 4) = f32x4{c[4], c[5], c[6], c[7]};
      }
      lds_wait();
      u32 old = 0; if (lane == 0) old = __hip_atomic_fetch_add((LAS u32*)&CTL[C_CNTF0 + pass], 1u, __ATOMIC_RELAXED, __HIP_MEMORY_SCOPE_WORKGROUP);
      old = __builtin_amdgcn_readfirstlane(old);
      if (old == 2) {
        asm volatile("" ::: "memory");
        if (lane < 4) {
          const f32x4 tg = *(LAS f32x4*)(RG + lane * 4) + *(LAS f32x4*)(RG + 16 + lane * 4) + *(LAS f32x4*)(RG + 32 + lane * 4);
          const f32x4 tu = *(LAS f32x4*)(RU + lane * 4) + *(LAS f32x4*)(RU + 16 + lane * 4) + *(LAS f32x4*)(RU + 32 + lane * 4);
          float h[4];
#pragma unroll
          for (int k = 0; k < 4; ++k) {
            const float z = rb(tg[k]);
            const float ge = rb(0.5f * z * (1.f + tanhf(0.7978845608028654f * (z + 0.044715f * z * z * z))));
            h[k] = ge * rb(tu[k]);
          }
          g_st(rws, WS_H + (u32)(cs * 8 + lane * 2) * 8, pk2(h[0], h[1]), pk2(h[2], h[3]), tag);
        }
      }
    }
    const int rsub = tau / 18, cc = tau - rsub * 18;
    const bool act = tau < 144;
    {
      u32x4 wd_[9];
#pragma unroll
      for (int i = 0; i < 9; ++i) wd_[i] = u32x4{0u, 0u, 0u, 0u};
#pragma unroll
      for (int i = 0; i < 8; ++i) wot[i] = u32x4{0u, 0u, 0u, 0u};
      if (act) {
#pragma unroll
        for (int i = 0; i < 9; ++i) wd_[i] = ldnt(p.wd + (size_t)(72 * rg + rsub + 8 * i) * DM + 144 * cg + cc * 8);
#pragma unroll
        for (int i = 0; i < 8; ++i) wot[i] = ldnt(p.wo + (size_t)(64 * rg + rsub + 8 * i) * DM + 144 * cg + cc * 8);
      }
      LAS float* HS = (LAS float*)(L8 + L_HS) + hw * 80;
      u32x4 hv; u32 sp = 0;
      for (;;) {
        hv = g_ld(rws, WS_H + (u32)(36 * rg + 2 * (lane < 18 ? lane : 0)) * 8);
        if (__all(hv[1] == tag && hv[3] == tag) || ++sp >= SPIN_GLB) break;
        __builtin_amdgcn_s_sleep(8);
      }
      if (lane < 18) *(LAS f32x4*)(HS + lane * 4) = f32x4{bflo(hv[0]), bfhi(hv[0]), bflo(hv[2]), bfhi(hv[2])};
      lds_wait();
      float a[8] = {0.f, 0.f, 0.f, 0.f, 0.f, 0.f, 0.f, 0.f};
#pragma unroll
      for (int i = 0; i < 9; ++i) fma8(a, wd_[i], HS[(rsub & 7) + 8 * i]);
      LAS float* RED = (LAS float*)(L8 + L_RED);
      if (act) { *(LAS f32x4*)(RED + rsub * 144 + cc * 8) = f32x4{a[0], a[1], a[2], a[3]}; *(LAS f32x4*)(RED + rsub * 144 + cc * 8 + 4) = f32x4{a[4], a[5], a[6], a[7]}; }
      lds_wait();
      u32 old = 0; if (lane == 0) old = __hip_atomic_fetch_add((LAS u32*)&CTL[C_CNTD], 1u, __ATOMIC_RELAXED, __HIP_MEMORY_SCOPE_WORKGROUP);
      old = __builtin_amdgcn_readfirstlane(old);
      if (old == 2) {
        asm volatile("" ::: "memory");
        for (int e = lane; e < 72; e += 64) {
          f32x2 t = {0.f, 0.f};
#pragma unroll
          for (int r = 0; r < 8; ++r) t += *(LAS f32x2*)(RED + r * 144 + 2 * e);
          g_st(rws, WS_F + (u32)(rg * DM + 144 * cg + 2 * e) * 8, __float_as_uint(t[0]), __float_as_uint(t[1]), tag);
        }
      }
    }
    {
      u32 sp = 0; while (CTL[C_QRDY] == 0 && ++sp < SPIN_LDS) __builtin_amdgcn_s_sleep(4);
      for (int x = NSTAT + hw;; x += NSCH) {
        sp = 0; while ((int)CTL[C_ISS] < x - AHEAD && ++sp < SPIN_LDS) __builtin_amdgcn_s_sleep(2);
        u32 t = 0;
        if (lane == 0) t = __hip_atomic_fetch_add((GAS u32*)(p.ws + WS_CNT + g * 256), 1u, __ATOMIC_RELAXED, __HIP_MEMORY_SCOPE_AGENT);
        t = __builtin_amdgcn_readfirstlane(t);
        if (t > 0xffffu) t = 0xffffu;
        CTL[C_TK + (x & 7)] = ((u32)(x + 1) << 16) | t;
        if (DYN0 + (int)t >= NT || sp >= SPIN_LDS) break;
      }
    }
  } else {
    const int ci = wave - 4;
    const int n = lane & 15, G = lane >> 4;
    u32 E = 0;
    if (ci == 0) E = __hip_atomic_load((GAS u32*)(p.ws + WS_E), __ATOMIC_RELAXED, __HIP_MEMORY_SCOPE_AGENT);
    __builtin_amdgcn_sched_barrier(0);
    wg_barrier();
    if (ci == 0) {
      const u32 tg = __builtin_amdgcn_readfirstlane(E) + 1u;
      u32x4 qv; u32 sp = 0;
      for (;;) {
        qv = g_ld(rws, WS_Q + (u32)(g * 128 + 2 * lane) * 8);
        if (__all(qv[1] == tg && qv[3] == tg) || ++sp >= SPIN_GLB) break;
        __builtin_amdgcn_s_sleep(2);
      }
      *(LAS u32x2*)(L8 + L_QS + lane * 8) = u32x2{qv[0], qv[2]};
      lds_wait();
      CTL[C_QRDY] = 1;
    } else {
      u32 sp = 0; while (CTL[C_QRDY] == 0 && ++sp < SPIN_LDS) __builtin_amdgcn_s_sleep(1);
    }
    asm volatile("" ::: "memory");
    bf16x8 qf[4];
#pragma unroll
    for (int i = 0; i < 4; ++i) {
      u32x4 t = *(LAS u32x4*)(L8 + L_QS + (n & 1) * 256 + i * 64 + G * 16);
      if (n >= 2) t = u32x4{0u, 0u, 0u, 0u};
      qf[i] = __builtin_bit_cast(bf16x8, t);
    }
    u32 ka[4], va[8];
#pragma unroll
    for (int i = 0; i < 4; ++i) ka[i] = n * 256 + (((4 * i + G) ^ n) << 4);
    {
      const u32 row = 4 * G + (n >> 2), pp = n & 3;
#pragma unroll
      for (int u = 0; u < 8; ++u) va[u] = 8192 + row * 256 + ((((2 * u + (pp >> 1)) ^ ((row & 7) << 1))) << 4) + (pp & 1) * 8;
    }
    f32x4 acc[8];
#pragma unroll
    for (int u = 0; u < 8; ++u) acc[u] = f32x4{0.f, 0.f, 0.f, 0.f};
    float m = -INFINITY, l = 0.f;
    int s = ci;
    for (int q = ci;; q += NCONS) {
      u32 pb, sp = 0;
      for (;;) { pb = CTL[C_PUB]; if ((int)(pb & 0x7fffffffu) > q || (pb >> 31) || ++sp >= SPIN_LDS) break; __builtin_amdgcn_s_sleep(1); }
      if ((int)(pb & 0x7fffffffu) <= q) break;
      asm volatile("" ::: "memory");
      LAS unsigned char* sb = L8 + s * SLOTB;
      bf16x8 kf[2][4]; s16x4 vf[8][2];
#pragma unroll
      for (int t = 0; t < 2; ++t)
#pragma unroll
        for (int i = 0; i < 4; ++i) kf[t][i] = *(LAS bf16x8*)(sb + ka[i] + t * 4096);
#pragma unroll
      for (int u = 0; u < 8; ++u)
#pragma unroll
        for (int t = 0; t < 2; ++t) vf[u][t] = __builtin_amdgcn_ds_read_tr16_b64_v4i16((LAS s16x4*)(sb + va[u] + t * 4096));
      f32x4 sc[2];
#pragma unroll
      for (int t = 0; t < 2; ++t) {
        sc[t] = f32x4{0.f, 0.f, 0.f, 0.f};
#pragma unroll
        for (int i = 0; i < 4; ++i) sc[t] = __builtin_amdgcn_mfma_f32_16x16x32_bf16(kf[t][i], qf[i], sc[t], 0, 0, 0);
      }
      asm volatile("s_waitcnt lgkmcnt(0)" : "+v"(vf[0][0]), "+v"(vf[0][1]), "+v"(vf[1][0]), "+v"(vf[1][1]), "+v"(vf[2][0]), "+v"(vf[2][1]), "+v"(vf[3][0]), "+v"(vf[3][1]),
                   "+v"(vf[4][0]), "+v"(vf[4][1]), "+v"(vf[5][0]), "+v"(vf[5][1]), "+v"(vf[6][0]), "+v"(vf[6][1]), "+v"(vf[7][0]), "+v"(vf[7][1]) :: "memory");
      CTL[C_FREE + s] = q + 1;
      float z[8];
#pragma unroll
      for (int t = 0; t < 2; ++t)
#pragma unroll
        for (int k = 0; k < 2; ++k) {
          const u32 a = pk2(sc[t][2 * k], sc[t][2 * k + 1]);
          const u32 c = pk2(bflo(a) * 0.08838834764831845f, bfhi(a) * 0.08838834764831845f);
          z[4 * t + 2 * k] = bflo(c) * 1.4426950408889634f; z[4 * t + 2 * k + 1] = bfhi(c) * 1.4426950408889634f;
        }
      const float zm = fmaxf(fmaxf(fmaxf(z[0], z[1]), fmaxf(z[2], z[3])), fmaxf(fmaxf(z[4], z[5]), fmaxf(z[6], z[7])));
      if (__any(zm > m)) {
        const float mn = fmaxf(m, ceilf(x32_max(x16_max(zm))));
        const float al = __builtin_amdgcn_exp2f(m - mn);
        l *= al; m = mn;
#pragma unroll
        for (int u = 0; u < 8; ++u) acc[u] *= al;
      }
      float pe[8];
#pragma unroll
      for (int k = 0; k < 8; ++k) pe[k] = __builtin_amdgcn_exp2f(z[k] - m);
      u32x4 pw = {pk2(pe[0], pe[1]), pk2(pe[2], pe[3]), pk2(pe[4], pe[5]), pk2(pe[6], pe[7])};
      l += ((pe[0] + pe[1]) + (pe[2] + pe[3])) + ((pe[4] + pe[5]) + (pe[6] + pe[7]));
      const bf16x8 pf = __builtin_bit_cast(bf16x8, pw);
#pragma unroll
      for (int u = 0; u < 8; ++u) {
        const auto vv = __builtin_shufflevector(vf[u][0], vf[u][1], 0, 1, 2, 3, 4, 5, 6, 7);
        acc[u] = __builtin_amdgcn_mfma_f32_16x16x32_bf16(__builtin_bit_cast(bf16x8, vv), pf, acc[u], 0, 0, 0);
      }
      s += NCONS; if (s >= NSLOT) s -= NSLOT;
    }
    l = x32_sum(x16_sum(l));
    LAS float* MR = (LAS float*)(L8 + L_MRG) + (ci * 2 + n) * 132;
    if (n < 2) {
#pragma unroll
      for (int u = 0; u < 8; ++u) *(LAS f32x4*)(MR + 16 * u + 4 * G) = acc[u];
      if (G == 0) { MR[128] = m; MR[129] = l; }
    }
  }
  wg_barrier();
  tag = CTL[C_TAG];
  if (tid < 130) {
    const int n = tid < 128 ? tid >> 6 : tid - 128, e = tid & 63;
    const LAS float* MR = (const LAS float*)(L8 + L_MRG) + n * 132;
    float mm[4], M = -INFINITY;
#pragma unroll
    for (int c = 0; c < 4; ++c) { mm[c] = MR[c * 264 + 128]; M = fmaxf(M, mm[c]); }
    f32x2 o = {0.f, 0.f}; float L = 0.f;
#pragma unroll
    for (int c = 0; c < 4; ++c) { const float w = __builtin_amdgcn_exp2f(mm[c] - M); o += w * *(const LAS f32x2*)(MR + c * 264 + 2 * e); L = fmaf(w, MR[c * 264 + 129], L); }
    const u32 base = WS_P + (u32)((2 * g + n) * 16 + wi) * P_STRIDE;
    if (tid < 128) g_st(rws, base + e * 16, __float_as_uint(o[0]), __float_as_uint(o[1]), tag);
    else g_st(rws, base + 1024, __float_as_uint(M), __float_as_uint(L), tag);
  }
  {
    const int hq = rg >> 1, j = tid >> 5, e = tid & 31;
    u32x4 av, ml; u32 sp = 0;
    for (;;) {
      av = g_ld(rws, WS_P + (u32)(hq * 16 + j) * P_STRIDE + (rg & 1) * 512 + e * 16);
      ml = g_ld(rws, WS_P + (u32)(hq * 16 + (lane & 15)) * P_STRIDE + 1024);
      if (__all(av[1] == tag && av[3] == tag && ml[1] == tag && ml[3] == tag) || ++sp >= SPIN_GLB) break;
      __builtin_amdgcn_s_sleep(1);
    }
    const float mj = __uint_as_float(ml[0]);
    const float M = row16_max(mj);
    const float wj = __builtin_amdgcn_exp2f(mj - M);
    const float L = row16_sum(wj * __uint_as_float(ml[2]));
    const float wa = __uint_as_float(__builtin_amdgcn_readlane(__float_as_uint(wj), 2 * wave)), wb = __uint_as_float(__builtin_amdgcn_readlane(__float_as_uint(wj), 2 * wave + 1));
    const float w = (lane < 32 ? wa : wb) / L;
    const float o0 = x32_sum(w * __uint_as_float(av[0])), o1 = x32_sum(w * __uint_as_float(av[2]));
    LAS float* OS = (LAS float*)(L8 + L_OS);
    if (lane < 32) { OS[(2 * e) * 8 + wave] = o0; OS[(2 * e + 1) * 8 + wave] = o1; }
  }
  wg_barrier();
  if (wave >= 1 && wave <= 3) {
    const int tau = tid - 64, rsub = tau / 18, cc = tau - rsub * 18;
    const LAS float* OS = (const LAS float*)(L8 + L_OS);
    float a[8] = {0.f, 0.f, 0.f, 0.f, 0.f, 0.f, 0.f, 0.f};
#pragma unroll
    for (int i = 0; i < 8; ++i) {
      const int row = (rsub & 7) + 8 * i;
      const f32x4 t = *(const LAS f32x4*)(OS + row * 8) + *(const LAS f32x4*)(OS + row * 8 + 4);
      fma8(a, wot[i], rb((t[0] + t[1]) + (t[2] + t[3])));
    }
    LAS float* RED = (LAS float*)(L8 + L_RED);
    if (tau < 144) { *(LAS f32x4*)(RED + rsub * 144 + cc * 8) = f32x4{a[0], a[1], a[2], a[3]}; *(LAS f32x4*)(RED + rsub * 144 + cc * 8 + 4) = f32x4{a[4], a[5], a[6], a[7]}; }
  }
  wg_barrier();
  if (tid < 72) {
    const LAS float* RED = (const LAS float*)(L8 + L_RED);
    f32x2 t = {0.f, 0.f};
#pragma unroll
    for (int r = 0; r < 8; ++r) t += *(const LAS f32x2*)(RED + r * 144 + 2 * tid);
    g_st(rws, WS_Y + (u32)(rg * DM + 144 * cg + 2 * tid) * 8, __float_as_uint(t[0]), __float_as_uint(t[1]), tag);
  }
  if (b >= 36) return;
  {
    const int r = tid >> 3, e = tid & 7;
    u32x4 yv, fv; u32 sp = 0;
    for (;;) {
      yv = g_ld(rws, WS_Y + (u32)(r * DM + 16 * b + 2 * e) * 8);
      fv = g_ld(rws, WS_F + (u32)(r * DM + 16 * b + 2 * e) * 8);
      if (__all(yv[1] == tag && yv[3] == tag && fv[1] == tag && fv[3] == tag) || ++sp >= SPIN_GLB) break;
      __builtin_amdgcn_s_sleep(1);
    }
    float t[4] = {__uint_as_float(yv[0]), __uint_as_float(yv[2]), __uint_as_float(fv[0]), __uint_as_float(fv[2])};
#pragma unroll
    for (int k = 0; k < 4; ++k) { t[k] += dpp<ROR8>(t[k]); t[k] = x32_sum(x16_sum(t[k])); }
    LAS float* FIN = (LAS float*)(L8 + L_FIN);
    if (lane < 8) *(LAS f32x4*)(FIN + (wave * 8 + lane) * 4) = f32x4{t[0], t[1], t[2], t[3]};
  }
  wg_barrier();
  if (tid < 8) {
    const LAS float* FIN = (const LAS float*)(L8 + L_FIN);
    f32x4 t = {0.f, 0.f, 0.f, 0.f};
#pragma unroll
    for (int w = 0; w < 8; ++w) t += *(const LAS f32x4*)(FIN + (w * 8 + tid) * 4);
    const u32 xx = *(const GAS u32*)(p.x + 16 * b + 2 * tid);
    float r[2];
#pragma unroll
    for (int k = 0; k < 2; ++k) {
      const float sv = rb(t[k]), f = rb(t[2 + k]);
      const float gt = rb(f / (1.f + __expf(-f)));
      r[k] = (k ? bfhi(xx) : bflo(xx)) + rb(sv * gt);
    }
    *(GAS u32*)(p.out + 16 * b + 2 * tid) = pk2(r[0], r[1]);
    if (b == 0 && tid == 0) __hip_atomic_store((GAS u32*)(p.ws + WS_E), tag, __ATOMIC_RELAXED, __HIP_MEMORY_SCOPE_AGENT);
  }
}
}

extern "C" void kernel_launch(void* const* d_in, const int* in_sizes, int n_in, void* d_out, int out_size, void* d_ws, size_t ws_size, hipStream_t stream) {
  fk::Params p;
  p.x = (const u16*)d_in[0]; p.k = (const u16*)d_in[1]; p.v = (const u16*)d_in[2]; p.wq = (const u16*)d_in[3]; p.wo = (const u16*)d_in[4];
  p.wg = (const u16*)d_in[6]; p.wu = (const u16*)d_in[7]; p.wd = (const u16*)d_in[8];
  p.out = (u16*)d_out; p.ws = (unsigned char*)d_ws;
  hipLaunchKernelGGL(fk::fused_decode, dim3(fk::NWG), dim3(fk::NTHR), 0, stream, p);
}
```

```cpp
#include <hip/hip_runtime.h>
#include <hip/hip_bf16.h>
#include <stdint.h>

#define LAS __attribute__((address_space(3)))
#define GAS __attribute__((address_space(1)))
typedef unsigned u32;
typedef unsigned long long u64;
typedef unsigned short u16;
typedef unsigned u32x4 __attribute__((ext_vector_type(4)));
typedef unsigned u32x2 __attribute__((ext_vector_type(2)));
typedef float f32x4 __attribute__((ext_vector_type(4)));
typedef float f32x2 __attribute__((ext_vector_type(2)));
typedef __bf16 bf16x2 __attribute__((ext_vector_type(2)));
typedef __bf16 bf16x8 __attribute__((ext_vector_type(8)));
typedef short s16x4 __attribute__((ext_vector_type(4)));

#define P_WN 20
#define P_NSLOT 8
#define P_NSTAT 120
#define P_AHEAD 3

namespace fk {
constexpr int DM = 576, NHKV = 16, SK = 65536, DH = 128, FF = 4608, NQ = 4096;
constexpr int NWG = 256, NTHR = 512;
constexpr int WN = P_WN, NSLOT = P_NSLOT, SLOTB = 16384, NCONS = 4, NSCH = 3;
constexpr int NT = SK / 32;
constexpr int NSTAT = P_NSTAT;
constexpr int DYN0 = NSTAT * 16;
constexpr int AHEAD = P_AHEAD;
static_assert(NSTAT <= NT / 16 && NSTAT >= NSLOT && AHEAD <= 7 && WN > 16 && WN <= 60, "knobs");
constexpr u32 WS_E = 0, WS_CNT = 4096, WS_Q = 16384;
constexpr u32 WS_G = WS_Q + 4 * NQ * 8, WS_U = WS_G + 4 * FF * 8;
constexpr u32 WS_P = WS_U + 4 * FF * 8;
constexpr u32 P_STRIDE = 144 * 8;
constexpr u32 WS_Y = WS_P + 32 * 16 * P_STRIDE, WS_F = WS_Y + 64 * DM * 8, WS_END = WS_F + 64 * DM * 8;
constexpr u32 L_CTL = NSLOT * SLOTB;
constexpr u32 L_QS = L_CTL + 256;
constexpr u32 L_RS = L_QS + 512;
constexpr u32 L_HS = L_RS + 5 * 3 * 64 * 4;
constexpr u32 L_RED = L_HS + 512;
constexpr u32 L_MRG = L_RED + 8 * 144 * 4;
constexpr u32 L_END = L_MRG + 4 * 2 * 132 * 4;
constexpr u32 L_OS = 0;
constexpr u32 L_FIN = 4096;
static_assert(L_END <= 163840 && L_END > 81920, "LDS");
enum { C_PUB = 0, C_ISS = 1, C_QRDY = 2, C_TAG = 3, C_CNTQ = 4, C_CNTF0 = 5, C_CNTF1 = 6, C_CNTD = 7, C_TK = 8, C_FREE = 16, C_HRDY = 40 };
constexpr u32 SPIN_LDS = 1u << 22, SPIN_GLB = 1u << 19;

struct Params {
  const u16 *x, *k, *v, *wq, *wo, *wg, *wu, *wd;
  u16* out;
  unsigned char* ws;
};

__device__ __forceinline__ float bflo(u32 u) { return __uint_as_float(u << 16); }
__device__ __forceinline__ float bfhi(u32 u) { return __uint_as_float(u & 0xffff0000u); }
__device__ __forceinline__ u32 pk2(float lo, float hi) { f32x2 f = {lo, hi}; return __builtin_bit_cast(u32, __builtin_convertvector(f, bf16x2)); }
__device__ __forceinline__ float rb(float f) { return bflo(pk2(f, 0.f) & 0xffffu); }
template <int CTRL> __device__ __forceinline__ float dpp(float x) {
  return __builtin_bit_cast(float, __builtin_amdgcn_mov_dpp(__builtin_bit_cast(int, x), CTRL, 0xf, 0xf, true));
}
constexpr int XOR1 = 0xB1, XOR2 = 0x4E, ROR4 = 0x124, ROR8 = 0x128;
__device__ __forceinline__ float x16_sum(float x) { auto s = __builtin_amdgcn_permlane16_swap(__float_as_uint(x), __float_as_uint(x), false, false); return __uint_as_float(s[0]) + __uint_as_float(s[1]); }
__device__ __forceinline__ float x32_sum(float x) { auto s = __builtin_amdgcn_permlane32_swap(__float_as_uint(x), __float_as_uint(x), false, false); return __uint_as_float(s[0]) + __uint_as_float(s[1]); }
__device__ __forceinline__ float x16_max(float x) { auto s = __builtin_amdgcn_permlane16_swap(__float_as_uint(x), __float_as_uint(x), false, false); return fmaxf(__uint_as_float(s[0]), __uint_as_float(s[1])); }
__device__ __forceinline__ float x32_max(float x) { auto s = __builtin_amdgcn_permlane32_swap(__float_as_uint(x), __float_as_uint(x), false, false); return fmaxf(__uint_as_float(s[0]), __uint_as_float(s[1])); }
__device__ __forceinline__ float row16_sum(float x) { x += dpp<XOR1>(x); x += dpp<XOR2>(x); x += dpp<ROR4>(x); x += dpp<ROR8>(x); return x; }
__device__ __forceinline__ float row16_max(float x) { x = fmaxf(x, dpp<XOR1>(x)); x = fmaxf(x, dpp<XOR2>(x)); x = fmaxf(x, dpp<ROR4>(x)); x = fmaxf(x, dpp<ROR8>(x)); return x; }
__device__ __forceinline__ float wave_sum(float x) { return x32_sum(x16_sum(row16_sum(x))); }
__device__ __forceinline__ void lds_wait() { asm volatile("s_waitcnt lgkmcnt(0)" ::: "memory"); }
__device__ __forceinline__ void vm_drain() { asm volatile("s_waitcnt vmcnt(0)" ::: "memory"); }
__device__ __forceinline__ void wg_barrier() { asm volatile("s_waitcnt lgkmcnt(0)\n\ts_barrier" ::: "memory"); }
__device__ __forceinline__ u32x4 ldnt(const u16* p) { return __builtin_nontemporal_load((const GAS u32x4*)p); }
__device__ __forceinline__ u32x4 mk_srd(const void* p, u32 bytes) {
  const u64 a = (u64)p;
  u32x4 r; r.x = __builtin_amdgcn_readfirstlane((u32)a); r.y = __builtin_amdgcn_readfirstlane((u32)(a >> 32) & 0xffffu); r.z = bytes; r.w = 0x00020000u; return r;
}
__device__ __forceinline__ u32x4 g_ld(__amdgpu_buffer_rsrc_t r, u32 off) { asm volatile("" ::: "memory"); return __builtin_amdgcn_raw_buffer_load_b128(r, off, 0, 16); }
__device__ __forceinline__ void g_st(__amdgpu_buffer_rsrc_t r, u32 off, u32 a, u32 b, u32 tag) { u32x4 v = {a, tag, b, tag}; __builtin_amdgcn_raw_buffer_store_b128(v, r, off, 0, 16); }

__device__ __forceinline__ void dma4(u32 ldsbase, u32 v0, u32 v1, u32 v2, u32 v3, u32x4 srd, u32 soff) {
  asm volatile("s_nop 4\n\ts_mov_b32 m0, %0\n\t"
               "s_waitcnt vmcnt(%c7)\n\t"
               "buffer_load_dwordx4 %1, %5, %6 offen nt lds\n\t"
               "s_waitcnt vmcnt(%c7)\n\t"
               "buffer_load_dwordx4 %2, %5, %6 offen offset:1024 nt lds\n\t"
               "s_waitcnt vmcnt(%c7)\n\t"
               "buffer_load_dwordx4 %3, %5, %6 offen offset:2048 nt lds\n\t"
               "s_waitcnt vmcnt(%c7)\n\t"
               "buffer_load_dwordx4 %4, %5, %6 offen offset:3072 nt lds"
               :: "s"(ldsbase), "v"(v0), "v"(v1), "v"(v2), "v"(v3), "s"(srd), "s"(soff), "i"(WN - 1) : "memory");
}

__device__ __forceinline__ void fma8(float (&acc)[8], u32x4 w, float xv) {
#pragma unroll
  for (int k = 0; k < 4; ++k) { acc[2 * k] = fmaf(xv, bflo(w[k]), acc[2 * k]); acc[2 * k + 1] = fmaf(xv, bfhi(w[k]), acc[2 * k + 1]); }
}
__device__ __forceinline__ float oct_sum(float x) { x += dpp<ROR8>(x); return x32_sum(x16_sum(x)); }

#define STAMP(i) do {} while (0)
#define STAMPV(i, v) do {} while (0)
#define RT() 0ull
__global__ void __launch_bounds__(NTHR, 2) fused_decode(Params p) {
  __shared__ __attribute__((aligned(16))) unsigned char lds[L_END];
  LAS unsigned char* L8 = (LAS unsigned char*)lds;
  volatile LAS u32* CTL = (volatile LAS u32*)(L8 + L_CTL);
  const int b = blockIdx.x, tid = threadIdx.x, lane = tid & 63;
  const int wave = __builtin_amdgcn_readfirstlane(tid >> 6);
  const int g = b >> 4, wi = b & 15;
  const int rg = (b & 7) | ((b >> 5) << 3), cg = (b >> 3) & 3;
  const __amdgpu_buffer_rsrc_t rws = __builtin_amdgcn_make_buffer_rsrc(p.ws, 0, WS_END, 0x00020000);
  u32 tag = 0;
  u32x4 wot[8];

  if (wave == 0) {
    __builtin_amdgcn_s_setprio(3);
    __builtin_amdgcn_s_waitcnt(0x0F70);
    const u32x4 rk = mk_srd(p.k + (size_t)g * SK * DH, SK * DH * 2), rv = mk_srd(p.v + (size_t)g * SK * DH, SK * DH * 2);
    const u32 r4 = lane >> 4, pc = lane & 15;
    const u32 vk0 = r4 * 256 + ((pc ^ r4) << 4), vk1 = r4 * 256 + ((pc ^ (4 + r4)) << 4), vk2 = r4 * 256 + ((pc ^ (8 + r4)) << 4), vk3 = r4 * 256 + ((pc ^ (12 + r4)) << 4);
    const u32 vv0 = r4 * 256 + ((pc ^ (r4 << 1)) << 4), vv1 = r4 * 256 + ((pc ^ ((4 + r4) << 1)) << 4);
    const u32 ldsb = (u32)(u64)L8;
    int q = 0, pub = 0, x = NSTAT, dead = 0, s = 0;
    u64 ts0 = RT(), ts8 = 0, ts32 = 0, ts64 = 0, ts96 = 0, tsS = 0, stF = 0, stT = 0, nF = 0, nT = 0;
    for (;; ++q) {
      int pos;
      if (q < NSTAT) pos = q * 16 + wi;
      else {
        bool fin = false;
        for (;;) {
          const int sc = (x - NSTAT) % NSCH;
          if (!((dead >> sc) & 1)) {
            u32 e = __builtin_amdgcn_readfirstlane(CTL[C_TK + (x & 7)]);
            if ((int)(e >> 16) != x + 1) {
              const u64 a_ = RT();
              vm_drain(); pub = q; CTL[C_PUB] = pub;
              u32 sp = 0;
              while ((int)((e = __builtin_amdgcn_readfirstlane(CTL[C_TK + (x & 7)])) >> 16) != x + 1 && ++sp < SPIN_LDS) __builtin_amdgcn_s_sleep(1);
              stT += RT() - a_; ++nT;
              if (sp >= SPIN_LDS) { fin = true; break; }
            }
            pos = DYN0 + (int)(e & 0xffffu);
            if (pos < NT) { ++x; break; }
            dead |= 1 << sc;
            if (dead == (1 << NSCH) - 1) { fin = true; break; }
          }
          ++x; CTL[C_ISS] = x;
        }
        if (fin) break;
      }
      if (q == 1) wg_barrier();
      if (q >= 1) CTL[C_ISS] = q < NSTAT ? q + 1 : x;
      if (q >= NSLOT) {
        const u32 need = q - NSLOT + 1;
        if (CTL[C_FREE + s] < need) {
          const u64 a_ = RT();
          vm_drain(); pub = q; CTL[C_PUB] = pub;
          u32 sp = 0; while (CTL[C_FREE + s] < need && ++sp < SPIN_LDS) __builtin_amdgcn_s_sleep(1);
          stF += RT() - a_; ++nF;
        }
      }
      const u32 so = __builtin_amdgcn_readfirstlane((u32)pos * 8192u);
      const u32 lb = __builtin_amdgcn_readfirstlane(ldsb + s * SLOTB);
#pragma unroll
      for (int gr = 0; gr < 4; ++gr) {
        if (gr < 2) dma4(lb + gr * 4096, vk0, vk1, vk2, vk3, rk, so + gr * 4096);
        else dma4(lb + gr * 4096, vv0, vv1, vv0, vv1, rv, so + (gr - 2) * 4096);
        if (((gr * 4 + 4 - WN) % 16 + 16) % 16 < 4) {
          const int done = q * 16 + gr * 4 + 4 - WN;
          const int c = done >= 0 ? done >> 4 : 0;
          if (c > pub) { pub = c; CTL[C_PUB] = pub; }
        }
      }
      s = s + 1 == NSLOT ? 0 : s + 1;
    }
    asm volatile("s_waitcnt vmcnt(16)" ::: "memory");
    if (q - 1 > pub) CTL[C_PUB] = q - 1;
    vm_drain();
    CTL[C_PUB] = (u32)q | 0x80000000u;
    CTL[C_ISS] = 0x40000000u;
    __builtin_amdgcn_s_setprio(0);
    STAMP(6); STAMPV(0, ts0); STAMPV(1, ts8); STAMPV(2, ts32); STAMPV(3, ts64); STAMPV(4, ts96); STAMPV(5, tsS); STAMPV(16, stF); STAMPV(17, nF); STAMPV(18, stT); STAMPV(19, nT); STAMPV(20, q);
    STAMPV(21, (unsigned)__builtin_amdgcn_s_getreg(20 | (3 << 11)) & 0xfu);
  } else if (wave <= 3) {
    const int hw = wave - 1, tau = tid - 64;
    const int ch = tau & 7, rr = tau >> 3;
    const int kq = b & 3, row0 = kq * 144 + rr;
    const int cq = b >> 2;
    const u32 E = __hip_atomic_load((GAS u32*)(p.ws + WS_E), __ATOMIC_RELAXED, __HIP_MEMORY_SCOPE_AGENT);
    if (hw == 0 && wi == 0 && lane == 0) __hip_atomic_store((GAS u32*)(p.ws + WS_CNT + g * 256), 0u, __ATOMIC_RELAXED, __HIP_MEMORY_SCOPE_AGENT);
    asm volatile("" ::: "memory");
    u16 xs[9], xm[6];
#pragma unroll
    for (int i = 0; i < 9; ++i) xs[i] = p.x[lane + 64 * i];
#pragma unroll
    for (int i = 0; i < 6; ++i) xm[i] = p.x[row0 + 24 * i];
    u32x4 wa_[6], wb_[6];
#pragma unroll
    for (int i = 0; i < 6; ++i) wa_[i] = ldnt(p.wq + (size_t)(row0 + 24 * i) * NQ + cq * 64 + ch * 8);
    if (hw == 0) CTL[lane] = 0;
    __builtin_amdgcn_sched_barrier(0);
    wg_barrier();
    __builtin_amdgcn_sched_barrier(0);
    tag = __builtin_amdgcn_readfirstlane(E) + 1u;
    if (hw == 0 && lane == 0) CTL[C_TAG] = tag;
    float sum = 0.f;
#pragma unroll
    for (int i = 0; i < 9; ++i) sum += bflo(xs[i]);
    const float mean = wave_sum(sum) * (1.f / DM);
    float var = 0.f;
#pragma unroll
    for (int i = 0; i < 9; ++i) { const float d = bflo(xs[i]) - mean; var = fmaf(d, d, var); }
    var = wave_sum(var) * (1.f / DM);
    const float mb = rb(mean), den = rb(sqrtf(rb(rb(var) + 1e-5f)));
    float xn[6];
#pragma unroll
    for (int i = 0; i < 6; ++i) xn[i] = rb(rb(bflo(xm[i]) - mb) / den);
    LAS float* RS = (LAS float*)(L8 + L_RS);
    {
      float a[8] = {0.f, 0.f, 0.f, 0.f, 0.f, 0.f, 0.f, 0.f};
#pragma unroll
      for (int i = 0; i < 6; ++i) fma8(a, wa_[i], xn[i]);
#pragma unroll
      for (int k = 0; k < 8; ++k) a[k] = oct_sum(a[k]);
      if (lane < 8) { *(LAS f32x4*)(RS + hw * 64 + lane * 8) = f32x4{a[0], a[1], a[2], a[3]}; *(LAS f32x4*)(RS + hw * 64 + lane * 8 + 4) = f32x4{a[4], a[5], a[6], a[7]}; }
      vm_drain();
      lds_wait();
      u32 old = 0; if (lane == 0) old = __hip_atomic_fetch_add((LAS u32*)&CTL[C_CNTQ], 1u, __ATOMIC_RELAXED, __HIP_MEMORY_SCOPE_WORKGROUP);
      old = __builtin_amdgcn_readfirstlane(old);
      if (old == 2) {
        asm volatile("" ::: "memory");
        if (lane < 32) {
          const f32x2 t = *(LAS f32x2*)(RS + lane * 2) + *(LAS f32x2*)(RS + 64 + lane * 2) + *(LAS f32x2*)(RS + 128 + lane * 2);
          g_st(rws, WS_Q + (u32)(kq * NQ + cq * 64 + lane * 2) * 8, __float_as_uint(t[0]), __float_as_uint(t[1]), tag);
        }
        STAMP(7);
      }
    }
#pragma unroll 1
    for (int pass = 0; pass < (b < 32 ? 2 : 1); ++pass) {
      const int cs = cq + 64 * pass;
#pragma unroll
      for (int i = 0; i < 6; ++i) { wa_[i] = ldnt(p.wg + (size_t)(row0 + 24 * i) * FF + cs * 64 + ch * 8); wb_[i] = ldnt(p.wu + (size_t)(row0 + 24 * i) * FF + cs * 64 + ch * 8); }
      float a[8] = {0.f, 0.f, 0.f, 0.f, 0.f, 0.f, 0.f, 0.f}, c[8] = {0.f, 0.f, 0.f, 0.f, 0.f, 0.f, 0.f, 0.f};
#pragma unroll
      for (int i = 0; i < 6; ++i) { fma8(a, wa_[i], xn[i]); fma8(c, wb_[i], xn[i]); }
#pragma unroll
      for (int k = 0; k < 8; ++k) { a[k] = oct_sum(a[k]); c[k] = oct_sum(c[k]); }
      LAS float* RG = RS + 192 + pass * 384; LAS float* RU = RG + 192;
      if (lane < 8) {
        *(LAS f32x4*)(RG + hw * 64 + lane * 8) = f32x4{a[0], a[1], a[2], a[3]}; *(LAS f32x4*)(RG + hw * 64 + lane * 8 + 4) = f32x4{a[4], a[5], a[6], a[7]};
        *(LAS f32x4*)(RU + hw * 64 + lane * 8) = f32x4{c[0], c[1], c[2], c[3]}; *(LAS f32x4*)(RU + hw * 64 + lane * 8 + 4) = f32x4{c[4], c[5], c[6], c[7]};
      }
      lds_wait();
      u32 old = 0; if (lane == 0) old = __hip_atomic_fetch_add((LAS u32*)&CTL[C_CNTF0 + pass], 1u, __ATOMIC_RELAXED, __HIP_MEMORY_SCOPE_WORKGROUP);
      old = __builtin_amdgcn_readfirstlane(old);
      if (old == 2) {
        asm volatile("" ::: "memory");
        const LAS float* R = (lane < 32 ? RG : RU) + (lane & 31) * 2;
        const f32x2 t = *(const LAS f32x2*)R + *(const LAS f32x2*)(R + 64) + *(const LAS f32x2*)(R + 128);
        g_st(rws, (lane < 32 ? WS_G : WS_U) + (u32)(kq * FF + cs * 64 + (lane & 31) * 2) * 8, __float_as_uint(t[0]), __float_as_uint(t[1]), tag);
      }
    }
    const int rsub = tau / 18, cc = tau - rsub * 18;
    const bool act = tau < 144;
    {
      u32x4 wd_[9];
#pragma unroll
      for (int i = 0; i < 9; ++i) wd_[i] = u32x4{0u, 0u, 0u, 0u};
#pragma unroll
      for (int i = 0; i < 8; ++i) wot[i] = u32x4{0u, 0u, 0u, 0u};
      if (act) {
#pragma unroll
        for (int i = 0; i < 9; ++i) wd_[i] = ldnt(p.wd + (size_t)(72 * rg + rsub + 8 * i) * DM + 144 * cg + cc * 8);
#pragma unroll
        for (int i = 0; i < 8; ++i) wot[i] = ldnt(p.wo + (size_t)(64 * rg + rsub + 8 * i) * DM + 144 * cg + cc * 8);
      }
      LAS float* HS = (LAS float*)(L8 + L_HS);
      if (hw == 2) {
        const u32 off = (u32)(72 * rg + 2 * (lane < 36 ? lane : 0)) * 8;
        u32x4 gv[4], uv[4]; u32 sp = 0;
        for (;;) {
          bool ok = true;
#pragma unroll
          for (int k = 0; k < 4; ++k) { gv[k] = g_ld(rws, WS_G + k * FF * 8 + off); uv[k] = g_ld(rws, WS_U + k * FF * 8 + off); }
#pragma unroll
          for (int k = 0; k < 4; ++k) ok = ok && gv[k][1] == tag && gv[k][3] == tag && uv[k][1] == tag && uv[k][3] == tag;
          if (__all(ok) || ++sp >= SPIN_GLB) break;
          __builtin_amdgcn_s_sleep(16);
        }
        f32x2 hh;
#pragma unroll
        for (int k = 0; k < 2; ++k) {
          const float z = rb((__uint_as_float(gv[0][2 * k]) + __uint_as_float(gv[1][2 * k])) + (__uint_as_float(gv[2][2 * k]) + __uint_as_float(gv[3][2 * k])));
          const float u = rb((__uint_as_float(uv[0][2 * k]) + __uint_as_float(uv[1][2 * k])) + (__uint_as_float(uv[2][2 * k]) + __uint_as_float(uv[3][2 * k])));
          const float ge = rb(0.5f * z * (1.f + tanhf(0.7978845608028654f * (z + 0.044715f * z * z * z))));
          hh[k] = rb(ge * u);
        }
        if (lane < 36) *(LAS f32x2*)(HS + lane * 2) = hh;
        lds_wait();
        CTL[C_HRDY] = 1;
      } else {
        u32 sp = 0; while (CTL[C_HRDY] == 0 && ++sp < SPIN_LDS) __builtin_amdgcn_s_sleep(8);
      }
      asm volatile("" ::: "memory");
      float a[8] = {0.f, 0.f, 0.f, 0.f, 0.f, 0.f, 0.f, 0.f};
#pragma unroll
      for (int i = 0; i < 9; ++i) fma8(a, wd_[i], HS[(rsub & 7) + 8 * i]);
      LAS float* RED = (LAS float*)(L8 + L_RED);
      if (act) { *(LAS f32x4*)(RED + rsub * 144 + cc * 8) = f32x4{a[0], a[1], a[2], a[3]}; *(LAS f32x4*)(RED + rsub * 144 + cc * 8 + 4) = f32x4{a[4], a[5], a[6], a[7]}; }
      lds_wait();
      u32 old = 0; if (lane == 0) old = __hip_atomic_fetch_add((LAS u32*)&CTL[C_CNTD], 1u, __ATOMIC_RELAXED, __HIP_MEMORY_SCOPE_WORKGROUP);
      old = __builtin_amdgcn_readfirstlane(old);
      if (old == 2) {
        asm volatile("" ::: "memory");
        for (int e = lane; e < 72; e += 64) {
          f32x2 t = {0.f, 0.f};
#pragma unroll
          for (int r = 0; r < 8; ++r) t += *(LAS f32x2*)(RED + r * 144 + 2 * e);
          g_st(rws, WS_F + (u32)(rg * DM + 144 * cg + 2 * e) * 8, __float_as_uint(t[0]), __float_as_uint(t[1]), tag);
        }
      }
    }
    if (hw == 0) STAMP(9);
    {
      u32 sp = 0; while (CTL[C_QRDY] == 0 && ++sp < SPIN_LDS) __builtin_amdgcn_s_sleep(4);
      for (int x = NSTAT + hw;; x += NSCH) {
        sp = 0; while ((int)CTL[C_ISS] < x - AHEAD && ++sp < SPIN_LDS) __builtin_amdgcn_s_sleep(2);
        u32 t = 0;
        if (lane == 0) t = __hip_atomic_fetch_add((GAS u32*)(p.ws + WS_CNT + g * 256), 1u, __ATOMIC_RELAXED, __HIP_MEMORY_SCOPE_AGENT);
        t = __builtin_amdgcn_readfirstlane(t);
        if (t > 0xffffu) t = 0xffffu;
        CTL[C_TK + (x & 7)] = ((u32)(x + 1) << 16) | t;
        if (DYN0 + (int)t >= NT || sp >= SPIN_LDS) break;
      }
    }
  } else {
    const int ci = wave - 4;
    const int n = lane & 15, G = lane >> 4;
    u32 E = 0;
    if (ci == 0) E = __hip_atomic_load((GAS u32*)(p.ws + WS_E), __ATOMIC_RELAXED, __HIP_MEMORY_SCOPE_AGENT);
    __builtin_amdgcn_sched_barrier(0);
    wg_barrier();
    if (ci == 0) {
      const u32 tg = __builtin_amdgcn_readfirstlane(E) + 1u;
      u32x4 qv[4][2]; u32 sp = 0;
      for (;;) {
        bool ok = true;
#pragma unroll
        for (int k = 0; k < 4; ++k)
#pragma unroll
          for (int j = 0; j < 2; ++j) qv[k][j] = g_ld(rws, WS_Q + (u32)(k * NQ + g * 256 + j * 128 + 2 * lane) * 8);
#pragma unroll
        for (int k = 0; k < 4; ++k)
#pragma unroll
          for (int j = 0; j < 2; ++j) ok = ok && qv[k][j][1] == tg && qv[k][j][3] == tg;
        if (__all(ok) || ++sp >= SPIN_GLB) break;
        __builtin_amdgcn_s_sleep(2);
      }
#pragma unroll
      for (int j = 0; j < 2; ++j) {
        const float q0 = (__uint_as_float(qv[0][j][0]) + __uint_as_float(qv[1][j][0])) + (__uint_as_float(qv[2][j][0]) + __uint_as_float(qv[3][j][0]));
        const float q1 = (__uint_as_float(qv[0][j][2]) + __uint_as_float(qv[1][j][2])) + (__uint_as_float(qv[2][j][2]) + __uint_as_float(qv[3][j][2]));
        *(LAS u32*)(L8 + L_QS + j * 256 + lane * 4) = pk2(q0, q1);
      }
      lds_wait();
      CTL[C_QRDY] = 1;
      STAMP(8);
    } else {
      u32 sp = 0; while (CTL[C_QRDY] == 0 && ++sp < SPIN_LDS) __builtin_amdgcn_s_sleep(1);
    }
    asm volatile("" ::: "memory");
    bf16x8 qf[4];
#pragma unroll
    for (int i = 0; i < 4; ++i) {
      u32x4 t = *(LAS u32x4*)(L8 + L_QS + (n & 1) * 256 + i * 64 + G * 16);
      if (n >= 2) t = u32x4{0u, 0u, 0u, 0u};
      qf[i] = __builtin_bit_cast(bf16x8, t);
    }
    u32 ka[4], va[8];
#pragma unroll
    for (int i = 0; i < 4; ++i) ka[i] = n * 256 + (((4 * i + G) ^ n) << 4);
    {
      const u32 row = 4 * G + (n >> 2), pp = n & 3;
#pragma unroll
      for (int u = 0; u < 8; ++u) va[u] = 8192 + row * 256 + ((((2 * u + (pp >> 1)) ^ ((row & 7) << 1))) << 4) + (pp & 1) * 8;
    }
    f32x4 acc[8];
#pragma unroll
    for (int u = 0; u < 8; ++u) acc[u] = f32x4{0.f, 0.f, 0.f, 0.f};
    float m = -INFINITY, l = 0.f;
    int s = ci;
    for (int q = ci;; q += NCONS) {
      u32 pb, sp = 0;
      for (;;) { pb = CTL[C_PUB]; if ((int)(pb & 0x7fffffffu) > q || (pb >> 31) || ++sp >= SPIN_LDS) break; __builtin_amdgcn_s_sleep(1); }
      if ((int)(pb & 0x7fffffffu) <= q) break;
      asm volatile("" ::: "memory");
      LAS unsigned char* sb = L8 + s * SLOTB;
      bf16x8 kf[2][4]; s16x4 vf[8][2];
#pragma unroll
      for (int t = 0; t < 2; ++t)
#pragma unroll
        for (int i = 0; i < 4; ++i) kf[t][i] = *(LAS bf16x8*)(sb + ka[i] + t * 4096);
#pragma unroll
      for (int u = 0; u < 8; ++u)
#pragma unroll
        for (int t = 0; t < 2; ++t) vf[u][t] = __builtin_amdgcn_ds_read_tr16_b64_v4i16((LAS s16x4*)(sb + va[u] + t * 4096));
      f32x4 sc[2];
#pragma unroll
      for (int t = 0; t < 2; ++t) {
        sc[t] = f32x4{0.f, 0.f, 0.f, 0.f};
#pragma unroll
        for (int i = 0; i < 4; ++i) sc[t] = __builtin_amdgcn_mfma_f32_16x16x32_bf16(kf[t][i], qf[i], sc[t], 0, 0, 0);
      }
      asm volatile("s_waitcnt lgkmcnt(0)" : "+v"(vf[0][0]), "+v"(vf[0][1]), "+v"(vf[1][0]), "+v"(vf[1][1]), "+v"(vf[2][0]), "+v"(vf[2][1]), "+v"(vf[3][0]), "+v"(vf[3][1]),
                   "+v"(vf[4][0]), "+v"(vf[4][1]), "+v"(vf[5][0]), "+v"(vf[5][1]), "+v"(vf[6][0]), "+v"(vf[6][1]), "+v"(vf[7][0]), "+v"(vf[7][1]) :: "memory");
      CTL[C_FREE + s] = q + 1;
      float z[8];
#pragma unroll
      for (int t = 0; t < 2; ++t)
#pragma unroll
        for (int k = 0; k < 2; ++k) {
          const u32 a = pk2(sc[t][2 * k], sc[t][2 * k + 1]);
          const u32 c = pk2(bflo(a) * 0.08838834764831845f, bfhi(a) * 0.08838834764831845f);
          z[4 * t + 2 * k] = bflo(c) * 1.4426950408889634f; z[4 * t + 2 * k + 1] = bfhi(c) * 1.4426950408889634f;
        }
      const float zm = fmaxf(fmaxf(fmaxf(z[0], z[1]), fmaxf(z[2], z[3])), fmaxf(fmaxf(z[4], z[5]), fmaxf(z[6], z[7])));
      if (__any(zm > m)) {
        const float mn = fmaxf(m, ceilf(x32_max(x16_max(zm))));
        const float al = __builtin_amdgcn_exp2f(m - mn);
        l *= al; m = mn;
#pragma unroll
        for (int u = 0; u < 8; ++u) acc[u] *= al;
      }
      float pe[8];
#pragma unroll
      for (int k = 0; k < 8; ++k) pe[k] = __builtin_amdgcn_exp2f(z[k] - m);
      u32x4 pw = {pk2(pe[0], pe[1]), pk2(pe[2], pe[3]), pk2(pe[4], pe[5]), pk2(pe[6], pe[7])};
      l += ((pe[0] + pe[1]) + (pe[2] + pe[3])) + ((pe[4] + pe[5]) + (pe[6] + pe[7]));
      const bf16x8 pf = __builtin_bit_cast(bf16x8, pw);
#pragma unroll
      for (int u = 0; u < 8; ++u) {
        const auto vv = __builtin_shufflevector(vf[u][0], vf[u][1], 0, 1, 2, 3, 4, 5, 6, 7);
        acc[u] = __builtin_amdgcn_mfma_f32_16x16x32_bf16(__builtin_bit_cast(bf16x8, vv), pf, acc[u], 0, 0, 0);
      }
      s += NCONS; if (s >= NSLOT) s -= NSLOT;
    }
    l = x32_sum(x16_sum(l));
    LAS float* MR = (LAS float*)(L8 + L_MRG) + (ci * 2 + n) * 132;
    if (n < 2) {
#pragma unroll
      for (int u = 0; u < 8; ++u) *(LAS f32x4*)(MR + 16 * u + 4 * G) = acc[u];
      if (G == 0) { MR[128] = m; MR[129] = l; }
    }
  }
  wg_barrier();
  tag = CTL[C_TAG];
  if (wave == 0) STAMP(10);
  if (tid < 130) {
    const int n = tid < 128 ? tid >> 6 : tid - 128, e = tid & 63;
    const LAS float* MR = (const LAS float*)(L8 + L_MRG) + n * 132;
    float mm[4], M = -INFINITY;
#pragma unroll
    for (int c = 0; c < 4; ++c) { mm[c] = MR[c * 264 + 128]; M = fmaxf(M, mm[c]); }
    f32x2 o = {0.f, 0.f}; float L = 0.f;
#pragma unroll
    for (int c = 0; c < 4; ++c) { const float w = __builtin_amdgcn_exp2f(mm[c] - M); o += w * *(const LAS f32x2*)(MR + c * 264 + 2 * e); L = fmaf(w, MR[c * 264 + 129], L); }
    const u32 base = WS_P + (u32)((2 * g + n) * 16 + wi) * P_STRIDE;
    if (tid < 128) g_st(rws, base + e * 16, __float_as_uint(o[0]), __float_as_uint(o[1]), tag);
    else g_st(rws, base + 1024, __float_as_uint(M), __float_as_uint(L), tag);
  }
  {
    const int hq = rg >> 1, j = tid >> 5, e = tid & 31;
    u32x4 av, ml; u32 sp = 0;
    for (;;) {
      av = g_ld(rws, WS_P + (u32)(hq * 16 + j) * P_STRIDE + (rg & 1) * 512 + e * 16);
      ml = g_ld(rws, WS_P + (u32)(hq * 16 + (lane & 15)) * P_STRIDE + 1024);
      if (__all(av[1] == tag && av[3] == tag && ml[1] == tag && ml[3] == tag) || ++sp >= SPIN_GLB) break;
      __builtin_amdgcn_s_sleep(1);
    }
    if (wave == 0) STAMP(11);
    const float mj = __uint_as_float(ml[0]);
    const float M = row16_max(mj);
    const float wj = __builtin_amdgcn_exp2f(mj - M);
    const float L = row16_sum(wj * __uint_as_float(ml[2]));
    const float wa = __uint_as_float(__builtin_amdgcn_readlane(__float_as_uint(wj), 2 * wave)), wb = __uint_as_float(__builtin_amdgcn_readlane(__float_as_uint(wj), 2 * wave + 1));
    const float w = (lane < 32 ? wa : wb) / L;
    const float o0 = x32_sum(w * __uint_as_float(av[0])), o1 = x32_sum(w * __uint_as_float(av[2]));
    LAS float* OS = (LAS float*)(L8 + L_OS);
    if (lane < 32) { OS[(2 * e) * 8 + wave] = o0; OS[(2 * e + 1) * 8 + wave] = o1; }
  }
  wg_barrier();
  if (wave >= 1 && wave <= 3) {
    const int tau = tid - 64, rsub = tau / 18, cc = tau - rsub * 18;
    const LAS float* OS = (const LAS float*)(L8 + L_OS);
    float a[8] = {0.f, 0.f, 0.f, 0.f, 0.f, 0.f, 0.f, 0.f};
#pragma unroll
    for (int i = 0; i < 8; ++i) {
      const int row = (rsub & 7) + 8 * i;
      const f32x4 t = *(const LAS f32x4*)(OS + row * 8) + *(const LAS f32x4*)(OS + row * 8 + 4);
      fma8(a, wot[i], rb((t[0] + t[1]) + (t[2] + t[3])));
    }
    LAS float* RED = (LAS float*)(L8 + L_RED);
    if (tau < 144) { *(LAS f32x4*)(RED + rsub * 144 + cc * 8) = f32x4{a[0], a[1], a[2], a[3]}; *(LAS f32x4*)(RED + rsub * 144 + cc * 8 + 4) = f32x4{a[4], a[5], a[6], a[7]}; }
  }
  wg_barrier();
  if (tid < 72) {
    const LAS float* RED = (const LAS float*)(L8 + L_RED);
    f32x2 t = {0.f, 0.f};
#pragma unroll
    for (int r = 0; r < 8; ++r) t += *(const LAS f32x2*)(RED + r * 144 + 2 * tid);
    g_st(rws, WS_Y + (u32)(rg * DM + 144 * cg + 2 * tid) * 8, __float_as_uint(t[0]), __float_as_uint(t[1]), tag);
  }
  if (wave == 0) STAMP(12);
  if (b >= 36) return;
  {
    const int r = tid >> 3, e = tid & 7;
    u32x4 yv, fv; u32 sp = 0;
    for (;;) {
      yv = g_ld(rws, WS_Y + (u32)(r * DM + 16 * b + 2 * e) * 8);
      fv = g_ld(rws, WS_F + (u32)(r * DM + 16 * b + 2 * e) * 8);
      if (__all(yv[1] == tag && yv[3] == tag && fv[1] == tag && fv[3] == tag) || ++sp >= SPIN_GLB) break;
      __builtin_amdgcn_s_sleep(1);
    }
    if (wave == 0) STAMP(13);
    float t[4] = {__uint_as_float(yv[0]), __uint_as_float(yv[2]), __uint_as_float(fv[0]), __uint_as_float(fv[2])};
#pragma unroll
    for (int k = 0; k < 4; ++k) { t[k] += dpp<ROR8>(t[k]); t[k] = x32_sum(x16_sum(t[k])); }
    LAS float* FIN = (LAS float*)(L8 + L_FIN);
    if (lane < 8) *(LAS f32x4*)(FIN + (wave * 8 + lane) * 4) = f32x4{t[0], t[1], t[2], t[3]};
  }
  wg_barrier();
  if (tid < 8) {
    const LAS float* FIN = (const LAS float*)(L8 + L_FIN);
    f32x4 t = {0.f, 0.f, 0.f, 0.f};
#pragma unroll
    for (int w = 0; w < 8; ++w) t += *(const LAS f32x4*)(FIN + (w * 8 + tid) * 4);
    const u32 xx = *(const GAS u32*)(p.x + 16 * b + 2 * tid);
    float r[2];
#pragma unroll
    for (int k = 0; k < 2; ++k) {
      const float sv = rb(t[k]), f = rb(t[2 + k]);
      const float gt = rb(f / (1.f + __expf(-f)));
      r[k] = (k ? bfhi(xx) : bflo(xx)) + rb(sv * gt);
    }
    *(GAS u32*)(p.out + 16 * b + 2 * tid) = pk2(r[0], r[1]);
    if (b == 0 && tid == 0) __hip_atomic_store((GAS u32*)(p.ws + WS_E), tag, __ATOMIC_RELAXED, __HIP_MEMORY_SCOPE_AGENT);
    STAMP(14);
  }
}
}

extern "C" void kernel_launch(void* const* d_in, const int* in_sizes, int n_in, void* d_out, int out_size, void* d_ws, size_t ws_size, hipStream_t stream) {
  fk::Params p;
  p.x = (const u16*)d_in[0]; p.k = (const u16*)d_in[1]; p.v = (const u16*)d_in[2]; p.wq = (const u16*)d_in[3]; p.wo = (const u16*)d_in[4];
  p.wg = (const u16*)d_in[6]; p.wu = (const u16*)d_in[7]; p.wd = (const u16*)d_in[8];
  p.out = (u16*)d_out; p.ws = (unsigned char*)d_ws;
  hipLaunchKernelGGL(fk::fused_decode, dim3(fk::NWG), dim3(fk::NTHR), 0, stream, p);
}
```

```cpp
#include <hip/hip_runtime.h>
#include <hip/hip_bf16.h>
#include <stdint.h>

#define LAS __attribute__((address_space(3)))
#define GAS __attribute__((address_space(1)))
typedef unsigned u32;
typedef unsigned long long u64;
typedef unsigned short u16;
typedef unsigned u32x4 __attribute__((ext_vector_type(4)));
typedef unsigned u32x2 __attribute__((ext_vector_type(2)));
typedef float f32x4 __attribute__((ext_vector_type(4)));
typedef float f32x2 __attribute__((ext_vector_type(2)));
typedef __bf16 bf16x2 __attribute__((ext_vector_type(2)));
typedef __bf16 bf16x8 __attribute__((ext_vector_type(8)));
typedef short s16x4 __attribute__((ext_vector_type(4)));

#define P_WN 22
#define P_DELAYF 0
#define P_NS 56
#define P_DLATE 3
#define P_OEARLY 1
#define P_WARM 1
#define P_WAT 24
#define P_PSTART 0
#define P_PSLEEP 1
#define P_LEAD 6

namespace fk {
constexpr int DM = 576, SK = 65536, DH = 128, FF = 4608, NQ = 4096;
constexpr int NWG = 256, NTHR = 512;
constexpr int WN = P_WN, NSLOT = 8, SLOTB = 16384, NCONS = 4;
constexpr int NS = P_NS;
constexpr int Q0 = 2 * NS;
constexpr int NDT = (64 - NS) * 32;
constexpr int NSCH = 3, LEAD = P_LEAD;
static_assert(WN > 16 && WN <= 39 && NS % 4 == 0 && NS >= 8 && NS < 64 && LEAD >= 5 && LEAD <= 10, "knobs (LEAD >= 5: the stream ends only after all three helpers have answered a ticket request)");
constexpr u32 WS_E = 0, WS_CNT = 4096, WS_Q = 8192;
constexpr u32 WS_G = WS_Q + 4 * NQ * 8, WS_U = WS_G + 4 * FF * 8;
constexpr u32 WS_P = WS_U + 4 * FF * 8;
constexpr u32 P_STRIDE = 128 * 8;
constexpr u32 WS_ML = WS_P + 32 * 16 * P_STRIDE;
constexpr u32 WS_Y = WS_ML + 32 * 16 * 16, WS_F = WS_Y + 64 * DM * 8, WS_END = WS_F + 72 * DM * 8;
constexpr u32 L_CTL = NSLOT * SLOTB;
constexpr u32 L_QS = L_CTL + 256;
constexpr u32 L_OS = L_QS + 512;
constexpr u32 L_RS = L_OS + 128;
constexpr u32 L_MRG = L_RS;
constexpr u32 L_SC = L_MRG + 4 * 2 * 132 * 4;
constexpr u32 L_STG = L_SC + NS * 256;
constexpr u32 L_END = L_SC + (P_DLATE < 2 ? NS * 256 + 32 * 384 : 64 * 384);
static_assert(L_END <= 163840 && L_END > 81920 && L_SC % 16 == 0, "LDS");
enum { C_PUB = 0, C_QRDY = 2, C_TAG = 3, C_CNTQ = 4, C_CNTF0 = 5, C_CNTF1 = 6, C_WRDY = 7, C_TK = 8, C_FREE = 16, C_ISS = 32, C_CNTV = 33, C_ORDY = 34, C_CNTM = 42 };
constexpr u32 SPIN_LDS = 1u << 22, SPIN_GLB = 1u << 19;

struct Params {
  const u16 *x, *k, *v, *wq, *wo, *wg, *wu, *wd;
  u16* out;
  unsigned char* ws;
};

__device__ __forceinline__ float asf(u32 u) { return __uint_as_float(u); }
__device__ __forceinline__ u32 asu(float f) { return __float_as_uint(f); }
__device__ __forceinline__ float bflo(u32 u) { return asf(u << 16); }
__device__ __forceinline__ float bfhi(u32 u) { return asf(u & 0xffff0000u); }
__device__ __forceinline__ u32 pk2(float lo, float hi) { f32x2 f = {lo, hi}; return __builtin_bit_cast(u32, __builtin_convertvector(f, bf16x2)); }
__device__ __forceinline__ float rb(float f) { return bflo(pk2(f, 0.f) & 0xffffu); }
template <int CTRL> __device__ __forceinline__ float dpp(float x) {
  return __builtin_bit_cast(float, __builtin_amdgcn_mov_dpp(__builtin_bit_cast(int, x), CTRL, 0xf, 0xf, true));
}
constexpr int XOR1 = 0xB1, XOR2 = 0x4E, ROR4 = 0x124, ROR8 = 0x128;
__device__ __forceinline__ float x16_sum(float x) { auto s = __builtin_amdgcn_permlane16_swap(asu(x), asu(x), false, false); return asf(s[0]) + asf(s[1]); }
__device__ __forceinline__ float x32_sum(float x) { auto s = __builtin_amdgcn_permlane32_swap(asu(x), asu(x), false, false); return asf(s[0]) + asf(s[1]); }
__device__ __forceinline__ float x16_max(float x) { auto s = __builtin_amdgcn_permlane16_swap(asu(x), asu(x), false, false); return fmaxf(asf(s[0]), asf(s[1])); }
__device__ __forceinline__ float x32_max(float x) { auto s = __builtin_amdgcn_permlane32_swap(asu(x), asu(x), false, false); return fmaxf(asf(s[0]), asf(s[1])); }
__device__ __forceinline__ float row16_sum(float x) { x += dpp<XOR1>(x); x += dpp<XOR2>(x); x += dpp<ROR4>(x); x += dpp<ROR8>(x); return x; }
__device__ __forceinline__ float row16_max(float x) { x = fmaxf(x, dpp<XOR1>(x)); x = fmaxf(x, dpp<XOR2>(x)); x = fmaxf(x, dpp<ROR4>(x)); x = fmaxf(x, dpp<ROR8>(x)); return x; }
__device__ __forceinline__ float wave_sum(float x) { return x32_sum(x16_sum(row16_sum(x))); }
__device__ __forceinline__ float oct_sum(float x) { x += dpp<ROR8>(x); return x32_sum(x16_sum(x)); }
__device__ __forceinline__ float quad_sum(float x) { x += dpp<ROR4>(x); x += dpp<ROR8>(x); return x32_sum(x16_sum(x)); }
__device__ __forceinline__ void lds_wait() { asm volatile("s_waitcnt lgkmcnt(0)" ::: "memory"); }
__device__ __forceinline__ void vm_drain() { asm volatile("s_waitcnt vmcnt(0)" ::: "memory"); }
__device__ __forceinline__ void wg_barrier() { asm volatile("s_waitcnt lgkmcnt(0)\n\ts_barrier" ::: "memory"); }
__device__ __forceinline__ u32x4 ldnt(const u16* p) { return __builtin_nontemporal_load((const GAS u32x4*)p); }
__device__ __forceinline__ u32x4 mk_srd(const void* p, u32 bytes) {
  const u64 a = (u64)p;
  u32x4 r; r.x = __builtin_amdgcn_readfirstlane((u32)a); r.y = __builtin_amdgcn_readfirstlane((u32)(a >> 32) & 0xffffu); r.z = bytes; r.w = 0x00020000u; return r;
}
__device__ __forceinline__ u32x4 g_ld(__amdgpu_buffer_rsrc_t r, u32 off) { asm volatile("" ::: "memory"); return __builtin_amdgcn_raw_buffer_load_b128(r, off, 0, 16); }
__device__ __forceinline__ void g_st(__amdgpu_buffer_rsrc_t r, u32 off, u32 a, u32 b, u32 tag) { u32x4 v = {a, tag, b, tag}; __builtin_amdgcn_raw_buffer_store_b128(v, r, off, 0, 16); }
__device__ __forceinline__ bool tg_ok(u32x4 v, u32 tag) { return v[1] == tag && v[3] == tag; }
__device__ __forceinline__ u32 lds_arrive(volatile LAS u32* w, int lane) {
  lds_wait();
  u32 old = 0; if (lane == 0) old = __hip_atomic_fetch_add((LAS u32*)w, 1u, __ATOMIC_RELAXED, __HIP_MEMORY_SCOPE_WORKGROUP);
  old = __builtin_amdgcn_readfirstlane(old);
  asm volatile("" ::: "memory");
  return old;
}

__device__ __forceinline__ void dma4(u32 ldsbase, u32 v0, u32 v1, u32 v2, u32 v3, u32x4 srd, u32 soff) {
  asm volatile("s_nop 4\n\ts_mov_b32 m0, %0\n\t"
               "s_waitcnt vmcnt(%c7)\n\t"
               "buffer_load_dwordx4 %1, %5, %6 offen nt lds\n\t"
               "s_waitcnt vmcnt(%c7)\n\t"
               "buffer_load_dwordx4 %2, %5, %6 offen offset:1024 nt lds\n\t"
               "s_waitcnt vmcnt(%c7)\n\t"
               "buffer_load_dwordx4 %3, %5, %6 offen offset:2048 nt lds\n\t"
               "s_waitcnt vmcnt(%c7)\n\t"
               "buffer_load_dwordx4 %4, %5, %6 offen offset:3072 nt lds"
               :: "s"(ldsbase), "v"(v0), "v"(v1), "v"(v2), "v"(v3), "s"(srd), "s"(soff), "i"(WN - 1) : "memory");
}

__device__ __forceinline__ void dma1(u32 ldsbase, u32 v0, u32x4 srd, u32 soff) {
  asm volatile("s_nop 4\n\ts_mov_b32 m0, %0\n\ts_nop 0\n\t"
               "buffer_load_dwordx4 %1, %2, %3 offen nt lds"
               :: "s"(ldsbase), "v"(v0), "s"(srd), "s"(soff) : "memory");
}

__device__ __forceinline__ void fma8(float (&acc)[8], u32x4 w, float xv) {
#pragma unroll
  for (int k = 0; k < 4; ++k) { acc[2 * k] = fmaf(xv, bflo(w[k]), acc[2 * k]); acc[2 * k + 1] = fmaf(xv, bfhi(w[k]), acc[2 * k + 1]); }
}

#define STAMPV(i, v) do {} while (0)
#define RT() 0ull

__global__ void __launch_bounds__(NTHR, 2) fused_decode(Params p) {
  __shared__ __attribute__((aligned(16))) unsigned char lds[L_END];
  LAS unsigned char* L8 = (LAS unsigned char*)lds;
  volatile LAS u32* CTL = (volatile LAS u32*)(L8 + L_CTL);
  const int b = blockIdx.x, tid = threadIdx.x, lane = tid & 63;
  const int wave = __builtin_amdgcn_readfirstlane(tid >> 6);
  const int g = b >> 4, wi = b & 15;
  const __amdgpu_buffer_rsrc_t rws = __builtin_amdgcn_make_buffer_rsrc(p.ws, 0, WS_END, 0x00020000);

  if (wave == 0) {
    __builtin_amdgcn_s_setprio(3);
    __builtin_amdgcn_s_waitcnt(0x0F70);
    const u32 r4 = lane >> 4, pc = lane & 15;
    const u32 ldsb = (u32)(u64)L8;
    u64 ts0 = RT(), ts8 = 0, ts32 = 0, ts64 = 0, ts96 = 0, tsS = 0, stF = 0, stT = 0;
    int pub = 0, q = 0;
    auto wait_free = [&](int s, u32 need) {
      const u64 a_ = RT();
      vm_drain(); pub = 2 * q; CTL[C_PUB] = pub;
      u32 sp = 0; while (CTL[C_FREE + s] < need && ++sp < SPIN_LDS) __builtin_amdgcn_s_sleep(1);
      stF += RT() - a_;
    };
    auto publish = [&](int gr) {
      if (((gr * 4 + 4 - WN) % 8 + 8) % 8 < 4) { pub = max(pub, (q * 16 + gr * 4 + 4 - WN) >> 3); CTL[C_PUB] = pub; }
    };
    const u32x4 rk = mk_srd(p.k + (size_t)g * SK * DH, SK * DH * 2), rv = mk_srd(p.v + (size_t)g * SK * DH, SK * DH * 2);
    const u32 vk0 = r4 * 256 + ((pc ^ r4) << 4), vk1 = r4 * 256 + ((pc ^ (4 + r4)) << 4), vk2 = vk0 ^ 128u, vk3 = vk1 ^ 128u;
    const u32 vv0 = r4 * 256 + ((pc ^ (r4 << 1)) << 4), vv1 = r4 * 256 + ((pc ^ ((4 + r4) << 1)) << 4);
#pragma unroll 1
    for (int ph = 0; ph < 2; ++ph) {
      const u32x4 rs = ph ? rv : rk;
      const u32 a0 = ph ? vv0 : vk0, a1 = ph ? vv1 : vk1, a2 = ph ? vv0 : vk2, a3 = ph ? vv1 : vk3;
#pragma unroll 1
      for (int i = 0; i < NS; ++i) {
        const u32 r = (u32)(wi + i) & 15u;
        const u32 so1 = ((u32)i << 17) | ((r >> 1) << 14) | ((r & 1u) << 12), so2 = so1 ^ 0x3000u;
        const u32 lb = ldsb + (q & (NSLOT - 1)) * SLOTB;
        dma4(lb, a0, a1, a2, a3, rs, so1);
        publish(0);
        if (q >= Q0 - 8) CTL[C_ISS] = 2 * q;
        dma4(lb + 4096, a0, a1, a2, a3, rs, so2);
        publish(1);
        if (q == 1) wg_barrier();
        const u32 fr = CTL[C_FREE + ((q + 1) & (NSLOT - 1))];
        dma4(lb + 8192, a0, a1, a2, a3, rs, so1 + SK * DH);
        publish(2);
        if (q >= Q0 - 8) CTL[C_ISS] = 2 * q + 1;
        const bool full = q >= NSLOT - 1 && (int)__builtin_amdgcn_readfirstlane(fr) < q + 2 - NSLOT;
        dma4(lb + 12288, a0, a1, a2, a3, rs, so2 + SK * DH);
        publish(3);
        ++q;
        if (__builtin_expect(full, 0)) wait_free(q & (NSLOT - 1), q + 1 - NSLOT);
      }
    }
    tsS = RT();
    {
      int x = Q0, sc = 0, xend = 0x7fffffff, dead = 0;
      u32 e = __builtin_amdgcn_readfirstlane(CTL[C_TK + (x & 7)]);
#pragma unroll 1
      for (;;) {
        if (__builtin_expect((int)(e >> 16) != x + 1 || (e & 0xffffu) >= (u32)NDT, 0)) {
          for (;;) {
            if (!((dead >> sc) & 1)) {
              e = __builtin_amdgcn_readfirstlane(CTL[C_TK + (x & 7)]);
              if ((int)(e >> 16) != x + 1) {
                const u64 a_ = RT();
                vm_drain(); pub = 2 * q; CTL[C_PUB] = pub;
                u32 sp = 0;
                while ((int)((e = __builtin_amdgcn_readfirstlane(CTL[C_TK + (x & 7)])) >> 16) != x + 1 && ++sp < SPIN_LDS) __builtin_amdgcn_s_sleep(1);
                stT += RT() - a_;
                if (sp >= SPIN_LDS) e = 0xffffu;
              }
              if ((e & 0xffffu) < (u32)NDT) break;
              dead |= 1 << sc;
              if (xend == 0x7fffffff) xend = (2 * x - 1 + LEAD) >> 1;
            }
            ++x; sc = sc == NSCH - 1 ? 0 : sc + 1;
            if (x > xend) break;
          }
        }
        if (x > xend) break;
        ++x; sc = sc == NSCH - 1 ? 0 : sc + 1;
        const u32 t = e & 0xffffu, tl = t >> 1;
        const u32 so1 = ((u32)NS << 17) + (((tl >> 1) << 14) | ((tl & 1u) << 12)) + (t & 1u) * (SK * DH), so2 = so1 ^ 0x3000u;
        const u32 lb = ldsb + (q & (NSLOT - 1)) * SLOTB;
        const bool ann = xend == 0x7fffffff;
        dma4(lb, vk0, vk1, vk2, vk3, rk, so1);
        publish(0);
        if (ann) CTL[C_ISS] = 2 * q;
        dma4(lb + 4096, vk0, vk1, vk2, vk3, rk, so2);
        publish(1);
        const u32 fr = CTL[C_FREE + ((q + 1) & (NSLOT - 1))];
        dma4(lb + 8192, vv0, vv1, vv0, vv1, rv, so1);
        publish(2);
        if (ann) CTL[C_ISS] = 2 * q + 1;
        const bool full = (int)__builtin_amdgcn_readfirstlane(fr) < q + 2 - NSLOT;
        const u32 en = CTL[C_TK + (x & 7)];
        dma4(lb + 12288, vv0, vv1, vv0, vv1, rv, so2);
        publish(3);
        e = __builtin_amdgcn_readfirstlane(en);
        ++q;
        if (__builtin_expect(full, 0)) wait_free(q & (NSLOT - 1), q + 1 - NSLOT);
        if (x > xend) break;
      }
    }
    CTL[C_ISS] = 0x40000000u + (u32)q;
    if (WN > 24) { asm volatile("s_waitcnt vmcnt(24)" ::: "memory"); pub = max(pub, 2 * q - 3); CTL[C_PUB] = pub; }
    asm volatile("s_waitcnt vmcnt(16)" ::: "memory"); pub = max(pub, 2 * q - 2); CTL[C_PUB] = pub;
    asm volatile("s_waitcnt vmcnt(8)" ::: "memory"); pub = max(pub, 2 * q - 1); CTL[C_PUB] = pub;
    vm_drain(); CTL[C_PUB] = 2 * q;
    const u64 tsE = RT();
    __builtin_amdgcn_s_setprio(0);
    u64 tsG = 0, tsO = 0;
    if (b < 72) {
      const u32 tag = CTL[C_TAG];
      const int r16 = lane >> 2, e = lane & 3;
      const u32 off = (u32)(r16 * DM + 8 * b + 2 * e) * 8;
      const u32 xx = *(const GAS u32*)(p.x + 8 * b + 2 * e);
      float gt[2] = {0.f, 0.f}, r[2] = {0.f, 0.f};
      int npass = 2; asm volatile("" : "+s"(npass));
#pragma unroll 1
      for (int pass = 0; pass < npass; ++pass) {
        const u32 base = (pass ? WS_Y : WS_F) + off;
        const bool five = pass == 0 && r16 < 8;
        u32x4 v[5]; u32 sp = 0;
        v[4] = u32x4{0u, tag, 0u, tag};
        for (bool ok = false;;) {
          if (!ok) {
#pragma unroll
            for (int i = 0; i < 4; ++i) v[i] = g_ld(rws, base + i * 16 * DM * 8);
            if (five) v[4] = g_ld(rws, base + 64 * DM * 8);
            ok = tg_ok(v[0], tag) && tg_ok(v[1], tag) && tg_ok(v[2], tag) && tg_ok(v[3], tag) && tg_ok(v[4], tag);
          }
          if (__all(ok) || ++sp >= SPIN_GLB) break;
          __builtin_amdgcn_s_sleep(1);
        }
        tsG = RT();
#pragma unroll
        for (int k = 0; k < 2; ++k) {
          const float f = rb(quad_sum(((asf(v[0][2 * k]) + asf(v[1][2 * k])) + (asf(v[2][2 * k]) + asf(v[3][2 * k]))) + asf(v[4][2 * k])));
          r[k] = (k ? bfhi(xx) : bflo(xx)) + rb(f * gt[k]);
          if (pass == 0) gt[k] = rb(f / (1.f + __expf(-f)));
        }
      }
      if (lane < 4) *(GAS u32*)(p.out + 8 * b + 2 * e) = pk2(r[0], r[1]);
      if (b == 0 && lane == 0) __hip_atomic_store((GAS u32*)(p.ws + WS_E), tag, __ATOMIC_RELAXED, __HIP_MEMORY_SCOPE_AGENT);
      tsO = RT();
    }
    STAMPV(0, ts0); STAMPV(1, ts8); STAMPV(2, ts32); STAMPV(3, ts64); STAMPV(4, ts96); STAMPV(5, tsS); STAMPV(6, tsE); STAMPV(13, tsG); STAMPV(14, tsO);
    STAMPV(16, stF); STAMPV(18, stT); STAMPV(20, q);
  } else if (wave <= 3) {
    const int hw = wave - 1, tau = tid - 64;
    const int ch = tau & 7, rr = tau >> 3;
    const int kq = b & 3, row0 = kq * 144 + rr;
    const int cq = b >> 2;
    const u32 E = __hip_atomic_load((GAS u32*)(p.ws + WS_E), __ATOMIC_RELAXED, __HIP_MEMORY_SCOPE_AGENT);
    if (hw == 0 && wi == 0 && lane == 0) __hip_atomic_store((GAS u32*)(p.ws + WS_CNT + g * 256), 0u, __ATOMIC_RELAXED, __HIP_MEMORY_SCOPE_AGENT);
    asm volatile("" ::: "memory");
    u16 xs[9], xm[6];
#pragma unroll
    for (int i = 0; i < 9; ++i) xs[i] = p.x[lane + 64 * i];
#pragma unroll
    for (int i = 0; i < 6; ++i) xm[i] = p.x[row0 + 24 * i];
    u32x4 wa_[6], wb_[6];
#pragma unroll
    for (int i = 0; i < 6; ++i) wa_[i] = ldnt(p.wq + (size_t)(row0 + 24 * i) * NQ + cq * 64 + ch * 8);
    if (hw == 0) CTL[lane] = 0;
    __builtin_amdgcn_sched_barrier(0);
    wg_barrier();
    __builtin_amdgcn_sched_barrier(0);
    const u32 tag = __builtin_amdgcn_readfirstlane(E) + 1u;
    if (hw == 0 && lane == 0) CTL[C_TAG] = tag;
    float sum = 0.f;
#pragma unroll
    for (int i = 0; i < 9; ++i) sum += bflo(xs[i]);
    const float mean = wave_sum(sum) * (1.f / DM);
    float var = 0.f;
#pragma unroll
    for (int i = 0; i < 9; ++i) { const float d = bflo(xs[i]) - mean; var = fmaf(d, d, var); }
    var = wave_sum(var) * (1.f / DM);
    const float mb = rb(mean), den = rb(sqrtf(rb(rb(var) + 1e-5f)));
    float xn[6];
#pragma unroll
    for (int i = 0; i < 6; ++i) xn[i] = rb(rb(bflo(xm[i]) - mb) / den);
    LAS float* RS = (LAS float*)(L8 + L_RS);
    u64 tsq = 0, tsh = 0, tsg = 0, tsp = 0;
    {
      float a[8] = {0.f, 0.f, 0.f, 0.f, 0.f, 0.f, 0.f, 0.f};
#pragma unroll
      for (int i = 0; i < 6; ++i) fma8(a, wa_[i], xn[i]);
#pragma unroll
      for (int k = 0; k < 8; ++k) a[k] = oct_sum(a[k]);
      if (lane < 8) { *(LAS f32x4*)(RS + hw * 64 + lane * 8) = f32x4{a[0], a[1], a[2], a[3]}; *(LAS f32x4*)(RS + hw * 64 + lane * 8 + 4) = f32x4{a[4], a[5], a[6], a[7]}; }
      vm_drain();
      if (lds_arrive(&CTL[C_CNTQ], lane) == 2) {
        if (lane < 32) {
          const f32x2 t = *(LAS f32x2*)(RS + lane * 2) + *(LAS f32x2*)(RS + 64 + lane * 2) + *(LAS f32x2*)(RS + 128 + lane * 2);
          g_st(rws, WS_Q + (u32)(kq * NQ + cq * 64 + lane * 2) * 8, asu(t[0]), asu(t[1]), tag);
        }
      }
      tsq = RT();
    }
    if (P_DELAYF) { u32 sp = 0; while (CTL[C_QRDY] == 0 && ++sp < SPIN_LDS) __builtin_amdgcn_s_sleep(4); }
#pragma unroll 1
    for (int pass = 0; pass < (b < 32 ? 2 : 1); ++pass) {
      const int cs = cq + 64 * pass;
#pragma unroll
      for (int i = 0; i < 6; ++i) { wa_[i] = ldnt(p.wg + (size_t)(row0 + 24 * i) * FF + cs * 64 + ch * 8); wb_[i] = ldnt(p.wu + (size_t)(row0 + 24 * i) * FF + cs * 64 + ch * 8); }
      float a[8] = {0.f, 0.f, 0.f, 0.f, 0.f, 0.f, 0.f, 0.f}, c[8] = {0.f, 0.f, 0.f, 0.f, 0.f, 0.f, 0.f, 0.f};
#pragma unroll
      for (int i = 0; i < 6; ++i) { fma8(a, wa_[i], xn[i]); fma8(c, wb_[i], xn[i]); }
#pragma unroll
      for (int k = 0; k < 8; ++k) { a[k] = oct_sum(a[k]); c[k] = oct_sum(c[k]); }
      LAS float* RG = RS + 192 + pass * 384; LAS float* RU = RG + 192;
      if (lane < 8) {
        *(LAS f32x4*)(RG + hw * 64 + lane * 8) = f32x4{a[0], a[1], a[2], a[3]}; *(LAS f32x4*)(RG + hw * 64 + lane * 8 + 4) = f32x4{a[4], a[5], a[6], a[7]};
        *(LAS f32x4*)(RU + hw * 64 + lane * 8) = f32x4{c[0], c[1], c[2], c[3]}; *(LAS f32x4*)(RU + hw * 64 + lane * 8 + 4) = f32x4{c[4], c[5], c[6], c[7]};
      }
      if (lds_arrive(&CTL[C_CNTF0 + pass], lane) == 2) {
        const LAS float* R = (lane < 32 ? RG : RU) + (lane & 31) * 2;
        const f32x2 t = *(const LAS f32x2*)R + *(const LAS f32x2*)(R + 64) + *(const LAS f32x2*)(R + 128);
        g_st(rws, (lane < 32 ? WS_G : WS_U) + (u32)(kq * FF + cs * 64 + (lane & 31) * 2) * 8, asu(t[0]), asu(t[1]), tag);
      }
    }
    tsh = RT();
    const bool ptask = hw == 0 ? b < 192 : (hw == 1 && b < 216);
    const int rg = hw == 0 ? (b & 63) : b % 72, cg = hw == 0 ? b >> 6 : b / 72;
    const u32x4 rw = mk_srd((hw == 0 ? p.wo : p.wd) + (size_t)64 * rg * DM + 192 * cg, ptask ? (63 * DM + 192) * 2 : 0);
    const u32 wa = (16 * ((lane >> 3) & 1) + 2 * (lane >> 4) + ((lane >> 2) & 1)) * 384 + (lane & 3) * 8;
    u32x4 af[2];
    s16x4 wlo[12][2], whi[12][2];
    auto stage = [&](u32 ldsoff, int row0, int nrow8) {
      u32 vo[3];
#pragma unroll
      for (int e = 0; e < 3; ++e) { const u32 u = (u32)(e * 64 + lane), row = u / 24u; vo[e] = row * (DM * 2) + (u - row * 24u) * 16u; }
#pragma unroll 1
      for (int r = 0; r < nrow8; ++r)
#pragma unroll
        for (int e = 0; e < 3; ++e) dma1((u32)(u64)L8 + ldsoff + r * 3072 + e * 1024, vo[e], rw, (u32)(row0 + 8 * r) * (DM * 2));
      vm_drain();
    };
    auto frags = [&](s16x4 (&w)[12][2], u32 ldsoff) {
#pragma unroll
      for (int t = 0; t < 12; ++t)
#pragma unroll
        for (int ks = 0; ks < 2; ++ks) w[t][ks] = __builtin_amdgcn_ds_read_tr16_b64_v4i16((LAS s16x4*)(L8 + ldsoff + wa + (8 * ks) * 384 + t * 32));
    };
    if (ptask) {
      u32 sp = 0; while ((int)CTL[C_PUB] < 2 * P_WAT && ++sp < SPIN_LDS) __builtin_amdgcn_s_sleep(16);
      if (hw == 1) {
        const u32 off = (u32)(64 * rg + 2 * (lane & 31)) * 8;
        u32x4 gv[4], uv[4];
        for (sp = 0;;) {
          bool ok = true;
#pragma unroll
          for (int k = 0; k < 4; ++k) { gv[k] = g_ld(rws, WS_G + k * FF * 8 + off); uv[k] = g_ld(rws, WS_U + k * FF * 8 + off); }
#pragma unroll
          for (int k = 0; k < 4; ++k) ok = ok && tg_ok(gv[k], tag) && tg_ok(uv[k], tag);
          if (__all(ok) || ++sp >= SPIN_GLB) break;
          __builtin_amdgcn_s_sleep(32);
        }
        float hh[2];
#pragma unroll
        for (int k = 0; k < 2; ++k) {
          const float z = rb((asf(gv[0][2 * k]) + asf(gv[1][2 * k])) + (asf(gv[2][2 * k]) + asf(gv[3][2 * k])));
          const float u = rb((asf(uv[0][2 * k]) + asf(uv[1][2 * k])) + (asf(uv[2][2 * k]) + asf(uv[3][2 * k])));
          const float ge = rb(0.5f * z * (1.f + tanhf(0.7978845608028654f * (z + 0.044715f * z * z * z))));
          hh[k] = ge * u;
        }
        const u32 hp = pk2(hh[0], hh[1]);
#pragma unroll
        for (int ks = 0; ks < 2; ++ks)
#pragma unroll
          for (int i = 0; i < 4; ++i) af[ks][i] = __builtin_amdgcn_ds_bpermute((8 * i + 4 * ks + (lane >> 4)) * 4, hp);
        if (!P_DLATE && b < 192) { sp = 0; while (CTL[C_WRDY] == 0 && ++sp < SPIN_LDS) __builtin_amdgcn_s_sleep(8); }
      }
      if ((hw == 0 && P_DLATE < 2) || !P_DLATE) {
        stage(L_STG, 0, 4); frags(wlo, L_STG); lds_wait();
        stage(L_STG, 32, 4); frags(whi, L_STG); lds_wait();
        CTL[C_WRDY] = 1;
      }
    }
    {
      u32 sp = 0; while (CTL[C_QRDY] == 0 && ++sp < SPIN_LDS) __builtin_amdgcn_s_sleep(4);
      for (int x = Q0 + hw;; x += NSCH) {
        int is; sp = 0;
        while ((is = (int)CTL[C_ISS]) < 2 * x - LEAD && ++sp < SPIN_LDS) { if (is == 0) __builtin_amdgcn_s_sleep(16); else __builtin_amdgcn_s_sleep(1); }
        if (is >= 0x40000000 || sp >= SPIN_LDS) break;
        u32 t = 0;
        if (lane == 0) t = __hip_atomic_fetch_add((GAS u32*)(p.ws + WS_CNT + g * 256), 1u, __ATOMIC_RELAXED, __HIP_MEMORY_SCOPE_AGENT);
        t = __builtin_amdgcn_readfirstlane(t);
        if (t > 0xffffu) t = 0xffffu;
        CTL[C_TK + (x & 7)] = ((u32)(x + 1) << 16) | t;
        if (t >= (u32)NDT) break;
      }
    }
    if (ptask && (P_DLATE >= 2 || (P_DLATE && hw == 1))) {
      u32 en = 0, sp = 0, lo = L_SC;
      if (hw == 1 || !P_OEARLY) while ((en = __builtin_amdgcn_readfirstlane(CTL[C_ISS])) < 0x40000000u && ++sp < SPIN_LDS) __builtin_amdgcn_s_sleep(1);
      if (hw == 1) {
        const int qa = ((int)(en - 0x40000000u) + 1) & ~1;
        sp = 0; while (((int)CTL[C_FREE + (qa & 7)] < qa - 7 || (int)CTL[C_FREE + (qa & 7) + 1] < qa - 6) && ++sp < SPIN_LDS) __builtin_amdgcn_s_sleep(1);
        if (P_DLATE == 3) { sp = 0; while ((int)CTL[C_PUB] < 2 * (int)(en - 0x40000000u) && ++sp < SPIN_LDS) __builtin_amdgcn_s_sleep(1); }
        lo = (u32)(qa & 7) * SLOTB;
      } else { sp = 0; while (CTL[C_CNTV] < (u32)NCONS && ++sp < SPIN_LDS) __builtin_amdgcn_s_sleep(1); }
      stage(lo, 0, 8); frags(wlo, lo); frags(whi, lo + 32 * 384);
      tsh = RT();
    }
    if (hw == 2 && b < 192) {
      const int rg = b & 63;
      u32 sp = 0;
      if (P_PSTART == 0) { while (CTL[C_ISS] < 0x40000000u && ++sp < SPIN_LDS) __builtin_amdgcn_s_sleep(2); }
      else { while (CTL[C_CNTM] < (u32)(P_PSTART == 1 ? NCONS : NCONS - 1) && ++sp < SPIN_LDS) __builtin_amdgcn_s_sleep(1); }
      const u32 off = WS_P + (u32)((rg >> 1) * 16 + (lane >> 3)) * P_STRIDE + (rg & 1) * 512 + (lane & 7) * 16;
      u32x4 d[8], ml;
      bool ok0 = false, ok1 = false, okm = false;
      for (sp = 0;;) {
        if (!okm) ml = g_ld(rws, WS_ML + (u32)((rg >> 1) * 16 + (lane & 15)) * 16);
        if (!ok0) {
#pragma unroll
          for (int i = 0; i < 4; ++i) d[i] = g_ld(rws, off + i * 128);
        }
        if (!ok1) {
#pragma unroll
          for (int i = 0; i < 4; ++i) d[4 + i] = g_ld(rws, off + 8 * P_STRIDE + i * 128);
        }
        okm = tg_ok(ml, tag);
        ok0 = tg_ok(d[0], tag) && tg_ok(d[1], tag) && tg_ok(d[2], tag) && tg_ok(d[3], tag);
        ok1 = tg_ok(d[4], tag) && tg_ok(d[5], tag) && tg_ok(d[6], tag) && tg_ok(d[7], tag);
        if (__all(okm && ok0 && ok1) || ++sp >= SPIN_GLB) break;
        __builtin_amdgcn_s_sleep(P_PSLEEP);
      }
      tsg = RT();
      const float mj = asf(ml[0]);
      const float wj = __builtin_amdgcn_exp2f(mj - row16_max(mj));
      const float sc = wj * __builtin_amdgcn_rcpf(row16_sum(wj * asf(ml[2])));
      u32x4 of;
      const float s0 = asf(__builtin_amdgcn_ds_bpermute((lane >> 3) * 4, asu(sc))), s1 = asf(__builtin_amdgcn_ds_bpermute((lane >> 3) * 4 + 32, asu(sc)));
#pragma unroll
      for (int i = 0; i < 4; ++i) {
        const u32 o = pk2(oct_sum(fmaf(s0, asf(d[i][0]), s1 * asf(d[4 + i][0]))), oct_sum(fmaf(s0, asf(d[i][2]), s1 * asf(d[4 + i][2]))));
        of[i] = o;
      }
      if (lane < 8) *(LAS u32x4*)(L8 + L_OS + lane * 16) = of;
      lds_wait();
      CTL[C_ORDY] = 1;
    }
    if (hw == 0 && ptask) {
      u32 sp = 0; while (CTL[C_ORDY] == 0 && ++sp < SPIN_LDS) {}
      asm volatile("" ::: "memory");
      af[0] = *(LAS u32x4*)(L8 + L_OS + (lane >> 4) * 16); af[1] = *(LAS u32x4*)(L8 + L_OS + 64 + (lane >> 4) * 16);
    }
    if (ptask) {
      float y[12];
#pragma unroll
      for (int t = 0; t < 12; ++t) {
        f32x4 acc = {0.f, 0.f, 0.f, 0.f};
#pragma unroll
        for (int ks = 0; ks < 2; ++ks)
          acc = __builtin_amdgcn_mfma_f32_16x16x32_bf16(__builtin_bit_cast(bf16x8, af[ks]), __builtin_bit_cast(bf16x8, __builtin_shufflevector(wlo[t][ks], whi[t][ks], 0, 1, 2, 3, 4, 5, 6, 7)), acc, 0, 0, 0);
        y[t] = acc[0];
      }
      const int G = lane >> 4;
      const bool odd = lane & 1;
      const float y0 = y[0], y1 = y[1], y2 = y[2], y3 = y[3], y4 = y[4], y5 = y[5], y6 = y[6], y7 = y[7], y8 = y[8], y9 = y[9], y10 = y[10], y11 = y[11];
      const float ya = (G & 2) ? ((G & 1) ? y6 : y4) : ((G & 1) ? y2 : y0), yb = (G & 2) ? ((G & 1) ? y7 : y5) : ((G & 1) ? y3 : y1);
      const float yc = (G & 1) ? y10 : y8, yd = (G & 1) ? y11 : y9;
      const float yan = dpp<XOR1>(ya), ybn = dpp<XOR1>(yb), ycn = dpp<XOR1>(yc), ydn = dpp<XOR1>(yd);
      const u32 base = (hw == 0 ? WS_Y : WS_F) + (u32)(rg * DM + 192 * cg + 32 * G + (odd ? 16 : 0) + (lane & 14)) * 8;
      g_st(rws, base, asu(odd ? ybn : ya), asu(odd ? yb : yan), tag);
      if (lane < 32) g_st(rws, base + 128 * 8, asu(odd ? ydn : yc), asu(odd ? yd : ycn), tag);
      tsp = RT();
    }
    if (hw == 0) { STAMPV(7, tsq); STAMPV(9, tsh); STAMPV(12, tsp); }
    if (hw == 2) STAMPV(11, tsg);
  } else {
    const int ci = wave - 4;
    const int n = lane & 15, G = lane >> 4;
    u32 E = 0;
    if (ci == 0) E = __hip_atomic_load((GAS u32*)(p.ws + WS_E), __ATOMIC_RELAXED, __HIP_MEMORY_SCOPE_AGENT);
    __builtin_amdgcn_sched_barrier(0);
    wg_barrier();
    u64 tsr = 0;
    if (ci == 0) {
      const u32 tg = __builtin_amdgcn_readfirstlane(E) + 1u;
      u32x4 qv[4][2]; u32 sp = 0;
      for (bool ok = false;;) {
        if (!ok) {
#pragma unroll
          for (int k = 0; k < 4; ++k)
#pragma unroll
            for (int j = 0; j < 2; ++j) qv[k][j] = g_ld(rws, WS_Q + (u32)(k * NQ + g * 256 + j * 128 + 2 * lane) * 8);
          ok = true;
#pragma unroll
          for (int k = 0; k < 4; ++k)
#pragma unroll
            for (int j = 0; j < 2; ++j) ok = ok && tg_ok(qv[k][j], tg);
        }
        if (__all(ok) || ++sp >= SPIN_GLB) break;
        __builtin_amdgcn_s_sleep(2);
      }
#pragma unroll
      for (int j = 0; j < 2; ++j) {
        const float q0 = (asf(qv[0][j][0]) + asf(qv[1][j][0])) + (asf(qv[2][j][0]) + asf(qv[3][j][0]));
        const float q1 = (asf(qv[0][j][2]) + asf(qv[1][j][2])) + (asf(qv[2][j][2]) + asf(qv[3][j][2]));
        *(LAS u32*)(L8 + L_QS + j * 256 + lane * 4) = pk2(q0, q1);
      }
      lds_wait();
      CTL[C_QRDY] = 1;
      tsr = RT();
    } else {
      u32 sp = 0; while (CTL[C_QRDY] == 0 && ++sp < SPIN_LDS) __builtin_amdgcn_s_sleep(1);
    }
    asm volatile("" ::: "memory");
    auto wait_pub = [&](int h) {
      u32 sp = 0;
      while ((int)CTL[C_PUB] < h && ++sp < SPIN_LDS) __builtin_amdgcn_s_sleep(1);
      asm volatile("" ::: "memory");
    };
    LAS unsigned char* SC = L8 + L_SC + ci * (NS / NCONS) * 256;
    const u32 tag = CTL[C_TAG];
    u32 ka[4], va[8];
#pragma unroll
    for (int i = 0; i < 4; ++i) ka[i] = n * 256 + (((4 * i + G) ^ n) << 4);
    float m, l;
    {
      bf16x8 qf[4][4];
#pragma unroll
      for (int i = 0; i < 4; ++i) {
        const u32x4 t4 = *(LAS u32x4*)(L8 + L_QS + (n & 1) * 256 + i * 64 + G * 16);
#pragma unroll
        for (int t = 0; t < 4; ++t) qf[t][i] = __builtin_bit_cast(bf16x8, (n >> 1) == t ? t4 : u32x4{0u, 0u, 0u, 0u});
      }
      const u32 sw = ((n >> 2) & 1) * 128 + (n & 1) * 64 + G * 16 + ((n >> 1) & 1) * 8;
      float mx = -INFINITY;
#pragma unroll 1
      for (int it = 0; it < NS / NCONS; ++it) {
        const int q = it * NCONS + ci, s = q & (NSLOT - 1);
        wait_pub(2 * q + 2);
        LAS unsigned char* sb = L8 + s * SLOTB;
        bf16x8 kf[4][4];
#pragma unroll
        for (int t = 0; t < 4; ++t)
#pragma unroll
          for (int i = 0; i < 4; ++i) kf[t][i] = *(LAS bf16x8*)(sb + ka[i] + t * 4096);
        f32x4 sa = {0.f, 0.f, 0.f, 0.f}, sb2 = {0.f, 0.f, 0.f, 0.f};
#pragma unroll
        for (int t = 0; t < 2; ++t)
#pragma unroll
          for (int i = 0; i < 4; ++i) sa = __builtin_amdgcn_mfma_f32_16x16x32_bf16(kf[t][i], qf[t][i], sa, 0, 0, 0);
        asm volatile("s_waitcnt lgkmcnt(0)" : "+v"(kf[2][0]), "+v"(kf[2][1]), "+v"(kf[2][2]), "+v"(kf[2][3]), "+v"(kf[3][0]), "+v"(kf[3][1]), "+v"(kf[3][2]), "+v"(kf[3][3]) :: "memory");
        CTL[C_FREE + s] = q + 1;
#pragma unroll
        for (int t = 2; t < 4; ++t)
#pragma unroll
          for (int i = 0; i < 4; ++i) sb2 = __builtin_amdgcn_mfma_f32_16x16x32_bf16(kf[t][i], qf[t][i], sb2, 0, 0, 0);
        const f32x4 sc = sa + sb2;
        const u32 a0 = pk2(sc[0], sc[1]), a1 = pk2(sc[2], sc[3]);
        const u32 c0 = pk2(bflo(a0) * 0.08838834764831845f, bfhi(a0) * 0.08838834764831845f), c1 = pk2(bflo(a1) * 0.08838834764831845f, bfhi(a1) * 0.08838834764831845f);
        mx = fmaxf(fmaxf(mx, fmaxf(bflo(c0), bfhi(c0))), fmaxf(bflo(c1), bfhi(c1)));
        if (n < 8) *(LAS u32x2*)(SC + it * 256 + sw) = u32x2{c0, c1};
      }
      mx = n < 8 ? mx : -INFINITY;
      mx = fmaxf(mx, dpp<XOR2>(mx)); mx = fmaxf(mx, dpp<ROR4>(mx)); mx = fmaxf(mx, dpp<ROR8>(mx)); mx = x32_max(x16_max(mx));
      m = ceilf(mx * 1.4426950408889634f);
      const float mo = dpp<XOR1>(m), mh = (((lane >> 2) ^ lane) & 1) ? mo : m;
      lds_wait();
      float ls = 0.f;
#pragma unroll
      for (int i = 0; i < (NS / NCONS + 3) / 4; ++i) {
        if ((NS / NCONS) % 4 != 0 && lane + 64 * i >= NS / NCONS * 16) break;
        LAS u32x4* cp = (LAS u32x4*)(SC + (lane + 64 * i) * 16);
        u32x4 z = *cp;
#pragma unroll
        for (int k = 0; k < 4; ++k) {
          const float p0 = __builtin_amdgcn_exp2f(fmaf(bflo(z[k]), 1.4426950408889634f, -mh)), p1 = __builtin_amdgcn_exp2f(fmaf(bfhi(z[k]), 1.4426950408889634f, -mh));
          ls += p0 + p1; z[k] = pk2(p0, p1);
        }
        *cp = z;
      }
      ls += dpp<XOR1>(ls); ls += dpp<XOR2>(ls); ls += dpp<ROR8>(ls); ls = x32_sum(x16_sum(ls));
      const float lo_ = dpp<ROR4>(ls);
      l = (((lane >> 2) ^ lane) & 1) ? lo_ : ls;
      lds_wait();
    }
    f32x4 acc[8];
    {
      const u32 row = 4 * G + (n >> 2), pp = n & 3;
#pragma unroll
      for (int u = 0; u < 8; ++u) { va[u] = row * 256 + ((((2 * u + (pp >> 1)) ^ ((row & 7) << 1))) << 4) + (pp & 1) * 8; acc[u] = f32x4{0.f, 0.f, 0.f, 0.f}; }
#pragma unroll 1
      for (int it = 0; it < NS / NCONS; ++it) {
        const int q = NS + it * NCONS + ci, s = q & (NSLOT - 1);
        const bf16x8 pf0 = *(LAS bf16x8*)(SC + it * 256 + (n & 1) * 64 + G * 16), pf1 = *(LAS bf16x8*)(SC + it * 256 + 128 + (n & 1) * 64 + G * 16);
        wait_pub(2 * q + 2);
        LAS unsigned char* sb = L8 + s * SLOTB;
        s16x4 vf[2][8][2];
#pragma unroll
        for (int h = 0; h < 2; ++h)
#pragma unroll
          for (int u = 0; u < 8; ++u)
#pragma unroll
            for (int t = 0; t < 2; ++t) vf[h][u][t] = __builtin_amdgcn_ds_read_tr16_b64_v4i16((LAS s16x4*)(sb + va[u] + (2 * h + t) * 4096));
#pragma unroll
        for (int u = 0; u < 8; ++u) acc[u] = __builtin_amdgcn_mfma_f32_16x16x32_bf16(__builtin_bit_cast(bf16x8, __builtin_shufflevector(vf[0][u][0], vf[0][u][1], 0, 1, 2, 3, 4, 5, 6, 7)), pf0, acc[u], 0, 0, 0);
        asm volatile("s_waitcnt lgkmcnt(0)" : "+v"(vf[1][0][0]), "+v"(vf[1][0][1]), "+v"(vf[1][1][0]), "+v"(vf[1][1][1]), "+v"(vf[1][2][0]), "+v"(vf[1][2][1]), "+v"(vf[1][3][0]), "+v"(vf[1][3][1]),
                     "+v"(vf[1][4][0]), "+v"(vf[1][4][1]), "+v"(vf[1][5][0]), "+v"(vf[1][5][1]), "+v"(vf[1][6][0]), "+v"(vf[1][6][1]), "+v"(vf[1][7][0]), "+v"(vf[1][7][1]) :: "memory");
        CTL[C_FREE + s] = q + 1;
#pragma unroll
        for (int u = 0; u < 8; ++u) acc[u] = __builtin_amdgcn_mfma_f32_16x16x32_bf16(__builtin_bit_cast(bf16x8, __builtin_shufflevector(vf[1][u][0], vf[1][u][1], 0, 1, 2, 3, 4, 5, 6, 7)), pf1, acc[u], 0, 0, 0);
      }
    }
    lds_arrive(&CTL[C_CNTV], lane);
    {
      bf16x8 qf[4];
#pragma unroll
      for (int i = 0; i < 4; ++i) qf[i] = *(LAS bf16x8*)(L8 + L_QS + (n & 1) * 256 + i * 64 + G * 16);
      float ld = 0.f;
#pragma unroll 1
      for (int q = Q0 + ci;; q += NCONS) {
        const int s = q & (NSLOT - 1);
        bool go;
        for (u32 sp = 0;;) {
          const u32 en = __builtin_amdgcn_readfirstlane(CTL[C_ISS]);
          go = (int)CTL[C_PUB] >= 2 * q + 1;
          if (go || (en >= 0x40000000u && q >= (int)(en - 0x40000000u)) || ++sp >= SPIN_LDS) break;
          __builtin_amdgcn_s_sleep(1);
        }
        if (!go) break;
        asm volatile("" ::: "memory");
        LAS unsigned char* sb = L8 + s * SLOTB;
        f32x4 sc[2];
#pragma unroll
        for (int t = 0; t < 2; ++t) {
          sc[t] = f32x4{0.f, 0.f, 0.f, 0.f};
#pragma unroll
          for (int i = 0; i < 4; ++i) sc[t] = __builtin_amdgcn_mfma_f32_16x16x32_bf16(*(LAS bf16x8*)(sb + ka[i] + t * 4096), qf[i], sc[t], 0, 0, 0);
        }
        float z[8];
#pragma unroll
        for (int t = 0; t < 2; ++t)
#pragma unroll
          for (int k = 0; k < 2; ++k) {
            const u32 a = pk2(sc[t][2 * k], sc[t][2 * k + 1]);
            const u32 c = pk2(bflo(a) * 0.08838834764831845f, bfhi(a) * 0.08838834764831845f);
            z[4 * t + 2 * k] = bflo(c) * 1.4426950408889634f; z[4 * t + 2 * k + 1] = bfhi(c) * 1.4426950408889634f;
          }
        const float zm = fmaxf(fmaxf(fmaxf(z[0], z[1]), fmaxf(z[2], z[3])), fmaxf(fmaxf(z[4], z[5]), fmaxf(z[6], z[7])));
        if (__any(zm > m)) {
          const float mn = fmaxf(m, ceilf(x32_max(x16_max(zm))));
          const float al = __builtin_amdgcn_exp2f(m - mn);
          l *= al; ld *= al; m = mn;
#pragma unroll
          for (int u = 0; u < 8; ++u) acc[u] *= al;
        }
        u32x4 pw;
#pragma unroll
        for (int k = 0; k < 4; ++k) { const float p0 = __builtin_amdgcn_exp2f(z[2 * k] - m), p1 = __builtin_amdgcn_exp2f(z[2 * k + 1] - m); ld += p0 + p1; pw[k] = pk2(p0, p1); }
        asm volatile("" : "+v"(pw));
        wait_pub(2 * q + 2);
        s16x4 vf[8][2];
#pragma unroll
        for (int u = 0; u < 8; ++u)
#pragma unroll
          for (int t = 0; t < 2; ++t) vf[u][t] = __builtin_amdgcn_ds_read_tr16_b64_v4i16((LAS s16x4*)(sb + va[u] + (2 + t) * 4096));
#pragma unroll
        for (int u = 0; u < 8; ++u) acc[u] = __builtin_amdgcn_mfma_f32_16x16x32_bf16(__builtin_bit_cast(bf16x8, __builtin_shufflevector(vf[u][0], vf[u][1], 0, 1, 2, 3, 4, 5, 6, 7)), __builtin_bit_cast(bf16x8, pw), acc[u], 0, 0, 0);
        lds_wait();
        CTL[C_FREE + s] = q + 1;
      }
      l += x32_sum(x16_sum(ld));
    }
    LAS float* MRG = (LAS float*)(L8 + L_MRG);
    if (n < 2) {
      LAS float* MR = MRG + (ci * 2 + n) * 132;
#pragma unroll
      for (int u = 0; u < 8; ++u) *(LAS f32x4*)(MR + 16 * u + 4 * G) = acc[u];
      if (G == 0) { MR[128] = m; MR[129] = l; }
    }
    if (lds_arrive(&CTL[C_CNTM], lane) == NCONS - 1) {
      const int hn = lane >> 5, e = lane & 31;
      const LAS float* MR = MRG + hn * 132;
      float mm[4], M = -INFINITY;
#pragma unroll
      for (int c = 0; c < 4; ++c) { mm[c] = MR[c * 264 + 128]; M = fmaxf(M, mm[c]); }
      f32x2 o0 = {0.f, 0.f}, o1 = {0.f, 0.f}; float L = 0.f;
#pragma unroll
      for (int c = 0; c < 4; ++c) {
        const float w = __builtin_amdgcn_exp2f(mm[c] - M);
        o0 += w * *(const LAS f32x2*)(MR + c * 264 + 2 * e); o1 += w * *(const LAS f32x2*)(MR + c * 264 + 64 + 2 * e); L = fmaf(w, MR[c * 264 + 129], L);
      }
      const u32 base = WS_P + (u32)((2 * g + hn) * 16 + wi) * P_STRIDE;
      g_st(rws, base + e * 16, asu(o0[0]), asu(o0[1]), tag);
      g_st(rws, base + 512 + e * 16, asu(o1[0]), asu(o1[1]), tag);
      if (e == 0) g_st(rws, WS_ML + (u32)((2 * g + hn) * 16 + wi) * 16, asu(M), asu(L), tag);
      STAMPV(10, RT());
    }
    if (ci == 0) STAMPV(8, tsr);
  }
}
}

extern "C" void kernel_launch(void* const* d_in, const int* in_sizes, int n_in, void* d_out, int out_size, void* d_ws, size_t ws_size, hipStream_t stream) {
  fk::Params p;
  p.x = (const u16*)d_in[0]; p.k = (const u16*)d_in[1]; p.v = (const u16*)d_in[2]; p.wq = (const u16*)d_in[3]; p.wo = (const u16*)d_in[4];
  p.wg = (const u16*)d_in[6]; p.wu = (const u16*)d_in[7]; p.wd = (const u16*)d_in[8];
  p.out = (u16*)d_out; p.ws = (unsigned char*)d_ws;
  hipLaunchKernelGGL(fk::fused_decode, dim3(fk::NWG), dim3(fk::NTHR), 0, stream, p);
}
```

```cpp
#include <hip/hip_runtime.h>
#include <hip/hip_bf16.h>
#include <stdint.h>

#define LAS __attribute__((address_space(3)))
#define GAS __attribute__((address_space(1)))
typedef unsigned u32;
typedef unsigned long long u64;
typedef unsigned short u16;
typedef unsigned u32x4 __attribute__((ext_vector_type(4)));
typedef unsigned u32x2 __attribute__((ext_vector_type(2)));
typedef float f32x4 __attribute__((ext_vector_type(4)));
typedef float f32x2 __attribute__((ext_vector_type(2)));
typedef __bf16 bf16x2 __attribute__((ext_vector_type(2)));
typedef __bf16 bf16x8 __attribute__((ext_vector_type(8)));
typedef short s16x4 __attribute__((ext_vector_type(4)));

#define P_WN 22
#define P_DELAYF 0
#define P_NS 56
#define P_DLATE 3
#define P_OEARLY 1
#define P_KP0 0
#define P_KP1 0
#define P_VP0 0
#define P_VP1 0
#define P_WNT 1
#define P_WARM 1
#define P_WAT 24
#define P_PSTART 0
#define P_PSLEEP 1
#define P_LEAD 6

namespace fk {
constexpr int DM = 576, SK = 65536, DH = 128, FF = 4608, NQ = 4096;
constexpr int NWG = 256, NTHR = 512;
constexpr int WN = P_WN, NSLOT = 8, SLOTB = 16384, NCONS = 4;
constexpr int NS = P_NS;
constexpr int Q0 = 2 * NS;
constexpr int NDT = (64 - NS) * 32;
constexpr int NSCH = 3, LEAD = P_LEAD;
static_assert(WN > 16 && WN <= 39 && NS % 4 == 0 && NS >= 8 && NS < 64 && LEAD >= 5 && LEAD <= 10, "knobs (LEAD >= 5: the stream ends only after all three helpers have answered a ticket request)");
constexpr u32 WS_E = 0, WS_CNT = 4096, WS_Q = 8192;
constexpr u32 WS_G = WS_Q + 4 * NQ * 8, WS_U = WS_G + 4 * FF * 8;
constexpr u32 WS_P = WS_U + 4 * FF * 8;
constexpr u32 P_STRIDE = 128 * 8;
constexpr u32 WS_ML = WS_P + 32 * 16 * P_STRIDE;
constexpr u32 WS_Y = WS_ML + 32 * 16 * 16, WS_F = WS_Y + 64 * DM * 8, WS_END = WS_F + 72 * DM * 8;
constexpr u32 L_CTL = NSLOT * SLOTB;
constexpr u32 L_QS = L_CTL + 256;
constexpr u32 L_OS = L_QS + 512;
constexpr u32 L_RS = L_OS + 128;
constexpr u32 L_MRG = L_RS;
constexpr u32 L_SC = L_MRG + 4 * 2 * 132 * 4;
constexpr u32 L_STG = L_SC + NS * 256;
constexpr u32 L_END = L_SC + (P_DLATE < 2 ? NS * 256 + 32 * 384 : 64 * 384);
static_assert(L_END <= 163840 && L_END > 81920 && L_SC % 16 == 0, "LDS");
enum { C_PUB = 0, C_QRDY = 2, C_TAG = 3, C_CNTQ = 4, C_CNTF0 = 5, C_CNTF1 = 6, C_WRDY = 7, C_TK = 8, C_FREE = 16, C_ISS = 32, C_CNTV = 33, C_ORDY = 34, C_CNTM = 42 };
constexpr u32 SPIN_LDS = 1u << 22, SPIN_GLB = 1u << 19;

struct Params {
  const u16 *x, *k, *v, *wq, *wo, *wg, *wu, *wd;
  u16* out;
  unsigned char* ws;
};

__device__ __forceinline__ float asf(u32 u) { return __uint_as_float(u); }
__device__ __forceinline__ u32 asu(float f) { return __float_as_uint(f); }
__device__ __forceinline__ float bflo(u32 u) { return asf(u << 16); }
__device__ __forceinline__ float bfhi(u32 u) { return asf(u & 0xffff0000u); }
__device__ __forceinline__ u32 pk2(float lo, float hi) { f32x2 f = {lo, hi}; return __builtin_bit_cast(u32, __builtin_convertvector(f, bf16x2)); }
__device__ __forceinline__ float rb(float f) { return bflo(pk2(f, 0.f) & 0xffffu); }
template <int CTRL> __device__ __forceinline__ float dpp(float x) {
  return __builtin_bit_cast(float, __builtin_amdgcn_mov_dpp(__builtin_bit_cast(int, x), CTRL, 0xf, 0xf, true));
}
constexpr int XOR1 = 0xB1, XOR2 = 0x4E, ROR4 = 0x124, ROR8 = 0x128;
__device__ __forceinline__ float x16_sum(float x) { auto s = __builtin_amdgcn_permlane16_swap(asu(x), asu(x), false, false); return asf(s[0]) + asf(s[1]); }
__device__ __forceinline__ float x32_sum(float x) { auto s = __builtin_amdgcn_permlane32_swap(asu(x), asu(x), false, false); return asf(s[0]) + asf(s[1]); }
__device__ __forceinline__ float x16_max(float x) { auto s = __builtin_amdgcn_permlane16_swap(asu(x), asu(x), false, false); return fmaxf(asf(s[0]), asf(s[1])); }
__device__ __forceinline__ float x32_max(float x) { auto s = __builtin_amdgcn_permlane32_swap(asu(x), asu(x), false, false); return fmaxf(asf(s[0]), asf(s[1])); }
__device__ __forceinline__ float row16_sum(float x) { x += dpp<XOR1>(x); x += dpp<XOR2>(x); x += dpp<ROR4>(x); x += dpp<ROR8>(x); return x; }
__device__ __forceinline__ float row16_max(float x) { x = fmaxf(x, dpp<XOR1>(x)); x = fmaxf(x, dpp<XOR2>(x)); x = fmaxf(x, dpp<ROR4>(x)); x = fmaxf(x, dpp<ROR8>(x)); return x; }
__device__ __forceinline__ float wave_sum(float x) { return x32_sum(x16_sum(row16_sum(x))); }
__device__ __forceinline__ float oct_sum(float x) { x += dpp<ROR8>(x); return x32_sum(x16_sum(x)); }
__device__ __forceinline__ float quad_sum(float x) { x += dpp<ROR4>(x); x += dpp<ROR8>(x); return x32_sum(x16_sum(x)); }
__device__ __forceinline__ void lds_wait() { asm volatile("s_waitcnt lgkmcnt(0)" ::: "memory"); }
__device__ __forceinline__ void vm_drain() { asm volatile("s_waitcnt vmcnt(0)" ::: "memory"); }
__device__ __forceinline__ void wg_barrier() { asm volatile("s_waitcnt lgkmcnt(0)\n\ts_barrier" ::: "memory"); }
__device__ __forceinline__ u32x4 ldnt(const u16* p) { return P_WNT ? __builtin_nontemporal_load((const GAS u32x4*)p) : *(const GAS u32x4*)p; }
__device__ __forceinline__ u32x4 mk_srd(const void* p, u32 bytes) {
  const u64 a = (u64)p;
  u32x4 r; r.x = __builtin_amdgcn_readfirstlane((u32)a); r.y = __builtin_amdgcn_readfirstlane((u32)(a >> 32) & 0xffffu); r.z = bytes; r.w = 0x00020000u; return r;
}
__device__ __forceinline__ u32x4 g_ld(__amdgpu_buffer_rsrc_t r, u32 off) { asm volatile("" ::: "memory"); return __builtin_amdgcn_raw_buffer_load_b128(r, off, 0, 16); }
__device__ __forceinline__ void g_st(__amdgpu_buffer_rsrc_t r, u32 off, u32 a, u32 b, u32 tag) { u32x4 v = {a, tag, b, tag}; __builtin_amdgcn_raw_buffer_store_b128(v, r, off, 0, 16); }
__device__ __forceinline__ bool tg_ok(u32x4 v, u32 tag) { return v[1] == tag && v[3] == tag; }
__device__ __forceinline__ u32 lds_arrive(volatile LAS u32* w, int lane) {
  lds_wait();
  u32 old = 0; if (lane == 0) old = __hip_atomic_fetch_add((LAS u32*)w, 1u, __ATOMIC_RELAXED, __HIP_MEMORY_SCOPE_WORKGROUP);
  old = __builtin_amdgcn_readfirstlane(old);
  asm volatile("" ::: "memory");
  return old;
}

#define DMA4_BODY(POL) \
  asm volatile("s_nop 4\n\ts_mov_b32 m0, %0\n\t" \
               "s_waitcnt vmcnt(%c7)\n\t" \
               "buffer_load_dwordx4 %1, %5, %6 offen " POL " lds\n\t" \
               "s_waitcnt vmcnt(%c7)\n\t" \
               "buffer_load_dwordx4 %2, %5, %6 offen offset:1024 " POL " lds\n\t" \
               "s_waitcnt vmcnt(%c7)\n\t" \
               "buffer_load_dwordx4 %3, %5, %6 offen offset:2048 " POL " lds\n\t" \
               "s_waitcnt vmcnt(%c7)\n\t" \
               "buffer_load_dwordx4 %4, %5, %6 offen offset:3072 " POL " lds" \
               :: "s"(ldsbase), "v"(v0), "v"(v1), "v"(v2), "v"(v3), "s"(srd), "s"(soff), "i"(WN - 1) : "memory")
__device__ __forceinline__ void dma4(u32 ldsbase, u32 v0, u32 v1, u32 v2, u32 v3, u32x4 srd, u32 soff, bool plain = false) {
  if (plain) DMA4_BODY(""); else DMA4_BODY("nt");
}
__device__ __forceinline__ void dma1(u32 ldsbase, u32 v0, u32x4 srd, u32 soff) {
  asm volatile("s_nop 4\n\ts_mov_b32 m0, %0\n\ts_nop 0\n\t"
               "buffer_load_dwordx4 %1, %2, %3 offen nt lds"
               :: "s"(ldsbase), "v"(v0), "s"(srd), "s"(soff) : "memory");
}

__device__ __forceinline__ void fma8(float (&acc)[8], u32x4 w, float xv) {
#pragma unroll
  for (int k = 0; k < 4; ++k) { acc[2 * k] = fmaf(xv, bflo(w[k]), acc[2 * k]); acc[2 * k + 1] = fmaf(xv, bfhi(w[k]), acc[2 * k + 1]); }
}

#define STAMPV(i, v) do {} while (0)
#define RT() 0ull

__global__ void __launch_bounds__(NTHR, 2) fused_decode(Params p) {
  __shared__ __attribute__((aligned(16))) unsigned char lds[L_END];
  LAS unsigned char* L8 = (LAS unsigned char*)lds;
  volatile LAS u32* CTL = (volatile LAS u32*)(L8 + L_CTL);
  const int b = blockIdx.x, tid = threadIdx.x, lane = tid & 63;
  const int wave = __builtin_amdgcn_readfirstlane(tid >> 6);
  const int g = b >> 4, wi = b & 15;
  const __amdgpu_buffer_rsrc_t rws = __builtin_amdgcn_make_buffer_rsrc(p.ws, 0, WS_END, 0x00020000);

  if (wave == 0) {
    __builtin_amdgcn_s_setprio(3);
    __builtin_amdgcn_s_waitcnt(0x0F70);
    const u32 r4 = lane >> 4, pc = lane & 15;
    const u32 ldsb = (u32)(u64)L8;
    u64 ts0 = RT(), ts8 = 0, ts32 = 0, ts64 = 0, ts96 = 0, tsS = 0, stF = 0, stT = 0;
    int pub = 0, q = 0;
    auto wait_free = [&](int s, u32 need) {
      const u64 a_ = RT();
      vm_drain(); pub = 2 * q; CTL[C_PUB] = pub;
      u32 sp = 0; while (CTL[C_FREE + s] < need && ++sp < SPIN_LDS) __builtin_amdgcn_s_sleep(1);
      stF += RT() - a_;
    };
    auto publish = [&](int gr) {
      if (((gr * 4 + 4 - WN) % 8 + 8) % 8 < 4) { pub = max(pub, (q * 16 + gr * 4 + 4 - WN) >> 3); CTL[C_PUB] = pub; }
    };
    const u32x4 rk = mk_srd(p.k + (size_t)g * SK * DH, SK * DH * 2), rv = mk_srd(p.v + (size_t)g * SK * DH, SK * DH * 2);
    const u32 vk0 = r4 * 256 + ((pc ^ r4) << 4), vk1 = r4 * 256 + ((pc ^ (4 + r4)) << 4), vk2 = vk0 ^ 128u, vk3 = vk1 ^ 128u;
    const u32 vv0 = r4 * 256 + ((pc ^ (r4 << 1)) << 4), vv1 = r4 * 256 + ((pc ^ ((4 + r4) << 1)) << 4);
#pragma unroll 1
    for (int ph = 0; ph < 2; ++ph) {
      const u32x4 rs = ph ? rv : rk;
      const u32 a0 = ph ? vv0 : vk0, a1 = ph ? vv1 : vk1, a2 = ph ? vv0 : vk2, a3 = ph ? vv1 : vk3;
      const bool pl0 = ph ? (2 * g >= P_VP0 && 2 * g < P_VP1) : (2 * g >= P_KP0 && 2 * g < P_KP1), pl1 = ph ? (2 * g + 1 >= P_VP0 && 2 * g + 1 < P_VP1) : (2 * g + 1 >= P_KP0 && 2 * g + 1 < P_KP1);
#pragma unroll 1
      for (int i = 0; i < NS; ++i) {
        const u32 r = (u32)(wi + i) & 15u;
        const u32 so1 = ((u32)i << 17) | ((r >> 1) << 14) | ((r & 1u) << 12), so2 = so1 ^ 0x3000u;
        const u32 lb = ldsb + (q & (NSLOT - 1)) * SLOTB;
        dma4(lb, a0, a1, a2, a3, rs, so1, pl0);
        publish(0);
        if (q >= Q0 - 8) CTL[C_ISS] = 2 * q;
        dma4(lb + 4096, a0, a1, a2, a3, rs, so2, pl0);
        publish(1);
        if (q == 1) wg_barrier();
        const u32 fr = CTL[C_FREE + ((q + 1) & (NSLOT - 1))];
        dma4(lb + 8192, a0, a1, a2, a3, rs, so1 + SK * DH, pl1);
        publish(2);
        if (q >= Q0 - 8) CTL[C_ISS] = 2 * q + 1;
        const bool full = q >= NSLOT - 1 && (int)__builtin_amdgcn_readfirstlane(fr) < q + 2 - NSLOT;
        dma4(lb + 12288, a0, a1, a2, a3, rs, so2 + SK * DH, pl1);
        publish(3);
        ++q;
        if (__builtin_expect(full, 0)) wait_free(q & (NSLOT - 1), q + 1 - NSLOT);
      }
    }
    tsS = RT();
    {
      int x = Q0, sc = 0, xend = 0x7fffffff, dead = 0;
      u32 e = __builtin_amdgcn_readfirstlane(CTL[C_TK + (x & 7)]);
#pragma unroll 1
      for (;;) {
        if (__builtin_expect((int)(e >> 16) != x + 1 || (e & 0xffffu) >= (u32)NDT, 0)) {
          for (;;) {
            if (!((dead >> sc) & 1)) {
              e = __builtin_amdgcn_readfirstlane(CTL[C_TK + (x & 7)]);
              if ((int)(e >> 16) != x + 1) {
                const u64 a_ = RT();
                vm_drain(); pub = 2 * q; CTL[C_PUB] = pub;
                u32 sp = 0;
                while ((int)((e = __builtin_amdgcn_readfirstlane(CTL[C_TK + (x & 7)])) >> 16) != x + 1 && ++sp < SPIN_LDS) __builtin_amdgcn_s_sleep(1);
                stT += RT() - a_;
                if (sp >= SPIN_LDS) e = 0xffffu;
              }
              if ((e & 0xffffu) < (u32)NDT) break;
              dead |= 1 << sc;
              if (xend == 0x7fffffff) xend = (2 * x - 1 + LEAD) >> 1;
            }
            ++x; sc = sc == NSCH - 1 ? 0 : sc + 1;
            if (x > xend) break;
          }
        }
        if (x > xend) break;
        ++x; sc = sc == NSCH - 1 ? 0 : sc + 1;
        const u32 t = e & 0xffffu, tl = t >> 1;
        const u32 so1 = ((u32)NS << 17) + (((tl >> 1) << 14) | ((tl & 1u) << 12)) + (t & 1u) * (SK * DH), so2 = so1 ^ 0x3000u;
        const u32 lb = ldsb + (q & (NSLOT - 1)) * SLOTB;
        const bool ann = xend == 0x7fffffff;
        dma4(lb, vk0, vk1, vk2, vk3, rk, so1);
        publish(0);
        if (ann) CTL[C_ISS] = 2 * q;
        dma4(lb + 4096, vk0, vk1, vk2, vk3, rk, so2);
        publish(1);
        const u32 fr = CTL[C_FREE + ((q + 1) & (NSLOT - 1))];
        dma4(lb + 8192, vv0, vv1, vv0, vv1, rv, so1);
        publish(2);
        if (ann) CTL[C_ISS] = 2 * q + 1;
        const bool full = (int)__builtin_amdgcn_readfirstlane(fr) < q + 2 - NSLOT;
        const u32 en = CTL[C_TK + (x & 7)];
        dma4(lb + 12288, vv0, vv1, vv0, vv1, rv, so2);
        publish(3);
        e = __builtin_amdgcn_readfirstlane(en);
        ++q;
        if (__builtin_expect(full, 0)) wait_free(q & (NSLOT - 1), q + 1 - NSLOT);
        if (x > xend) break;
      }
    }
    CTL[C_ISS] = 0x40000000u + (u32)q;
    if (WN > 24) { asm volatile("s_waitcnt vmcnt(24)" ::: "memory"); pub = max(pub, 2 * q - 3); CTL[C_PUB] = pub; }
    asm volatile("s_waitcnt vmcnt(16)" ::: "memory"); pub = max(pub, 2 * q - 2); CTL[C_PUB] = pub;
    asm volatile("s_waitcnt vmcnt(8)" ::: "memory"); pub = max(pub, 2 * q - 1); CTL[C_PUB] = pub;
    vm_drain(); CTL[C_PUB] = 2 * q;
    const u64 tsE = RT();
    __builtin_amdgcn_s_setprio(0);
    u64 tsG = 0, tsO = 0;
    if (b < 72) {
      const u32 tag = CTL[C_TAG];
      const int r16 = lane >> 2, e = lane & 3;
      const u32 off = (u32)(r16 * DM + 8 * b + 2 * e) * 8;
      const u32 xx = *(const GAS u32*)(p.x + 8 * b + 2 * e);
      float gt[2] = {0.f, 0.f}, r[2] = {0.f, 0.f};
      int npass = 2; asm volatile("" : "+s"(npass));
#pragma unroll 1
      for (int pass = 0; pass < npass; ++pass) {
        const u32 base = (pass ? WS_Y : WS_F) + off;
        const bool five = pass == 0 && r16 < 8;
        u32x4 v[5]; u32 sp = 0;
        v[4] = u32x4{0u, tag, 0u, tag};
        for (bool ok = false;;) {
          if (!ok) {
#pragma unroll
            for (int i = 0; i < 4; ++i) v[i] = g_ld(rws, base + i * 16 * DM * 8);
            if (five) v[4] = g_ld(rws, base + 64 * DM * 8);
            ok = tg_ok(v[0], tag) && tg_ok(v[1], tag) && tg_ok(v[2], tag) && tg_ok(v[3], tag) && tg_ok(v[4], tag);
          }
          if (__all(ok) || ++sp >= SPIN_GLB) break;
          __builtin_amdgcn_s_sleep(1);
        }
        tsG = RT();
#pragma unroll
        for (int k = 0; k < 2; ++k) {
          const float f = rb(quad_sum(((asf(v[0][2 * k]) + asf(v[1][2 * k])) + (asf(v[2][2 * k]) + asf(v[3][2 * k]))) + asf(v[4][2 * k])));
          r[k] = (k ? bfhi(xx) : bflo(xx)) + rb(f * gt[k]);
          if (pass == 0) gt[k] = rb(f / (1.f + __expf(-f)));
        }
      }
      if (lane < 4) *(GAS u32*)(p.out + 8 * b + 2 * e) = pk2(r[0], r[1]);
      if (b == 0 && lane == 0) __hip_atomic_store((GAS u32*)(p.ws + WS_E), tag, __ATOMIC_RELAXED, __HIP_MEMORY_SCOPE_AGENT);
      tsO = RT();
    }
    STAMPV(0, ts0); STAMPV(1, ts8); STAMPV(2, ts32); STAMPV(3, ts64); STAMPV(4, ts96); STAMPV(5, tsS); STAMPV(6, tsE); STAMPV(13, tsG); STAMPV(14, tsO);
    STAMPV(16, stF); STAMPV(18, stT); STAMPV(20, q);
  } else if (wave <= 3) {
    const int hw = wave - 1, tau = tid - 64;
    const int ch = tau & 7, rr = tau >> 3;
    const int kq = b & 3, row0 = kq * 144 + rr;
    const int cq = b >> 2;
    const u32 E = __hip_atomic_load((GAS u32*)(p.ws + WS_E), __ATOMIC_RELAXED, __HIP_MEMORY_SCOPE_AGENT);
    if (hw == 0 && wi == 0 && lane == 0) __hip_atomic_store((GAS u32*)(p.ws + WS_CNT + g * 256), 0u, __ATOMIC_RELAXED, __HIP_MEMORY_SCOPE_AGENT);
    asm volatile("" ::: "memory");
    u16 xs[9], xm[6];
#pragma unroll
    for (int i = 0; i < 9; ++i) xs[i] = p.x[lane + 64 * i];
#pragma unroll
    for (int i = 0; i < 6; ++i) xm[i] = p.x[row0 + 24 * i];
    u32x4 wa_[6], wb_[6];
#pragma unroll
    for (int i = 0; i < 6; ++i) wa_[i] = ldnt(p.wq + (size_t)(row0 + 24 * i) * NQ + cq * 64 + ch * 8);
    if (hw == 0) CTL[lane] = 0;
    __builtin_amdgcn_sched_barrier(0);
    wg_barrier();
    __builtin_amdgcn_sched_barrier(0);
    const u32 tag = __builtin_amdgcn_readfirstlane(E) + 1u;
    if (hw == 0 && lane == 0) CTL[C_TAG] = tag;
    float sum = 0.f;
#pragma unroll
    for (int i = 0; i < 9; ++i) sum += bflo(xs[i]);
    const float mean = wave_sum(sum) * (1.f / DM);
    float var = 0.f;
#pragma unroll
    for (int i = 0; i < 9; ++i) { const float d = bflo(xs[i]) - mean; var = fmaf(d, d, var); }
    var = wave_sum(var) * (1.f / DM);
    const float mb = rb(mean), den = rb(sqrtf(rb(rb(var) + 1e-5f)));
    float xn[6];
#pragma unroll
    for (int i = 0; i < 6; ++i) xn[i] = rb(rb(bflo(xm[i]) - mb) / den);
    LAS float* RS = (LAS float*)(L8 + L_RS);
    u64 tsq = 0, tsh = 0, tsg = 0, tsp = 0;
    {
      float a[8] = {0.f, 0.f, 0.f, 0.f, 0.f, 0.f, 0.f, 0.f};
#pragma unroll
      for (int i = 0; i < 6; ++i) fma8(a, wa_[i], xn[i]);
#pragma unroll
      for (int k = 0; k < 8; ++k) a[k] = oct_sum(a[k]);
      if (lane < 8) { *(LAS f32x4*)(RS + hw * 64 + lane * 8) = f32x4{a[0], a[1], a[2], a[3]}; *(LAS f32x4*)(RS + hw * 64 + lane * 8 + 4) = f32x4{a[4], a[5], a[6], a[7]}; }
      vm_drain();
      if (lds_arrive(&CTL[C_CNTQ], lane) == 2) {
        if (lane < 32) {
          const f32x2 t = *(LAS f32x2*)(RS + lane * 2) + *(LAS f32x2*)(RS + 64 + lane * 2) + *(LAS f32x2*)(RS + 128 + lane * 2);
          g_st(rws, WS_Q + (u32)(kq * NQ + cq * 64 + lane * 2) * 8, asu(t[0]), asu(t[1]), tag);
        }
      }
      tsq = RT();
    }
    if (P_DELAYF) { u32 sp = 0; while (CTL[C_QRDY] == 0 && ++sp < SPIN_LDS) __builtin_amdgcn_s_sleep(4); }
#pragma unroll 1
    for (int pass = 0; pass < (b < 32 ? 2 : 1); ++pass) {
      const int cs = cq + 64 * pass;
#pragma unroll
      for (int i = 0; i < 6; ++i) { wa_[i] = ldnt(p.wg + (size_t)(row0 + 24 * i) * FF + cs * 64 + ch * 8); wb_[i] = ldnt(p.wu + (size_t)(row0 + 24 * i) * FF + cs * 64 + ch * 8); }
      float a[8] = {0.f, 0.f, 0.f, 0.f, 0.f, 0.f, 0.f, 0.f}, c[8] = {0.f, 0.f, 0.f, 0.f, 0.f, 0.f, 0.f, 0.f};
#pragma unroll
      for (int i = 0; i < 6; ++i) { fma8(a, wa_[i], xn[i]); fma8(c, wb_[i], xn[i]); }
#pragma unroll
      for (int k = 0; k < 8; ++k) { a[k] = oct_sum(a[k]); c[k] = oct_sum(c[k]); }
      LAS float* RG = RS + 192 + pass * 384; LAS float* RU = RG + 192;
      if (lane < 8) {
        *(LAS f32x4*)(RG + hw * 64 + lane * 8) = f32x4{a[0], a[1], a[2], a[3]}; *(LAS f32x4*)(RG + hw * 64 + lane * 8 + 4) = f32x4{a[4], a[5], a[6], a[7]};
        *(LAS f32x4*)(RU + hw * 64 + lane * 8) = f32x4{c[0], c[1], c[2], c[3]}; *(LAS f32x4*)(RU + hw * 64 + lane * 8 + 4) = f32x4{c[4], c[5], c[6], c[7]};
      }
      if (lds_arrive(&CTL[C_CNTF0 + pass], lane) == 2) {
        const LAS float* R = (lane < 32 ? RG : RU) + (lane & 31) * 2;
        const f32x2 t = *(const LAS f32x2*)R + *(const LAS f32x2*)(R + 64) + *(const LAS f32x2*)(R + 128);
        g_st(rws, (lane < 32 ? WS_G : WS_U) + (u32)(kq * FF + cs * 64 + (lane & 31) * 2) * 8, asu(t[0]), asu(t[1]), tag);
      }
    }
    tsh = RT();
    const bool ptask = hw == 0 ? b < 192 : (hw == 1 && b < 216);
    const int rg = hw == 0 ? (b & 63) : b % 72, cg = hw == 0 ? b >> 6 : b / 72;
    const u32x4 rw = mk_srd((hw == 0 ? p.wo : p.wd) + (size_t)64 * rg * DM + 192 * cg, ptask ? (63 * DM + 192) * 2 : 0);
    const u32 wa = (16 * ((lane >> 3) & 1) + 2 * (lane >> 4) + ((lane >> 2) & 1)) * 384 + (lane & 3) * 8;
    u32x4 af[2];
    s16x4 wlo[12][2], whi[12][2];
    auto stage = [&](u32 ldsoff, int row0, int nrow8) {
      u32 vo[3];
#pragma unroll
      for (int e = 0; e < 3; ++e) { const u32 u = (u32)(e * 64 + lane), row = u / 24u; vo[e] = row * (DM * 2) + (u - row * 24u) * 16u; }
#pragma unroll 1
      for (int r = 0; r < nrow8; ++r)
#pragma unroll
        for (int e = 0; e < 3; ++e) dma1((u32)(u64)L8 + ldsoff + r * 3072 + e * 1024, vo[e], rw, (u32)(row0 + 8 * r) * (DM * 2));
      vm_drain();
    };
    auto frags = [&](s16x4 (&w)[12][2], u32 ldsoff) {
#pragma unroll
      for (int t = 0; t < 12; ++t)
#pragma unroll
        for (int ks = 0; ks < 2; ++ks) w[t][ks] = __builtin_amdgcn_ds_read_tr16_b64_v4i16((LAS s16x4*)(L8 + ldsoff + wa + (8 * ks) * 384 + t * 32));
    };
    if (ptask) {
      u32 sp = 0; while ((int)CTL[C_PUB] < 2 * P_WAT && ++sp < SPIN_LDS) __builtin_amdgcn_s_sleep(16);
      if (hw == 1) {
        const u32 off = (u32)(64 * rg + 2 * (lane & 31)) * 8;
        u32x4 gv[4], uv[4];
        for (sp = 0;;) {
          bool ok = true;
#pragma unroll
          for (int k = 0; k < 4; ++k) { gv[k] = g_ld(rws, WS_G + k * FF * 8 + off); uv[k] = g_ld(rws, WS_U + k * FF * 8 + off); }
#pragma unroll
          for (int k = 0; k < 4; ++k) ok = ok && tg_ok(gv[k], tag) && tg_ok(uv[k], tag);
          if (__all(ok) || ++sp >= SPIN_GLB) break;
          __builtin_amdgcn_s_sleep(32);
        }
        float hh[2];
#pragma unroll
        for (int k = 0; k < 2; ++k) {
          const float z = rb((asf(gv[0][2 * k]) + asf(gv[1][2 * k])) + (asf(gv[2][2 * k]) + asf(gv[3][2 * k])));
          const float u = rb((asf(uv[0][2 * k]) + asf(uv[1][2 * k])) + (asf(uv[2][2 * k]) + asf(uv[3][2 * k])));
          const float ge = rb(0.5f * z * (1.f + tanhf(0.7978845608028654f * (z + 0.044715f * z * z * z))));
          hh[k] = ge * u;
        }
        const u32 hp = pk2(hh[0], hh[1]);
#pragma unroll
        for (int ks = 0; ks < 2; ++ks)
#pragma unroll
          for (int i = 0; i < 4; ++i) af[ks][i] = __builtin_amdgcn_ds_bpermute((8 * i + 4 * ks + (lane >> 4)) * 4, hp);
        if (!P_DLATE && b < 192) { sp = 0; while (CTL[C_WRDY] == 0 && ++sp < SPIN_LDS) __builtin_amdgcn_s_sleep(8); }
      }
      if ((hw == 0 && P_DLATE < 2) || !P_DLATE) {
        stage(L_STG, 0, 4); frags(wlo, L_STG); lds_wait();
        stage(L_STG, 32, 4); frags(whi, L_STG); lds_wait();
        CTL[C_WRDY] = 1;
      }
    }
    {
      u32 sp = 0; while (CTL[C_QRDY] == 0 && ++sp < SPIN_LDS) __builtin_amdgcn_s_sleep(4);
      for (int x = Q0 + hw;; x += NSCH) {
        int is; sp = 0;
        while ((is = (int)CTL[C_ISS]) < 2 * x - LEAD && ++sp < SPIN_LDS) { if (is == 0) __builtin_amdgcn_s_sleep(16); else __builtin_amdgcn_s_sleep(1); }
        if (is >= 0x40000000 || sp >= SPIN_LDS) break;
        u32 t = 0;
        if (lane == 0) t = __hip_atomic_fetch_add((GAS u32*)(p.ws + WS_CNT + g * 256), 1u, __ATOMIC_RELAXED, __HIP_MEMORY_SCOPE_AGENT);
        t = __builtin_amdgcn_readfirstlane(t);
        if (t > 0xffffu) t = 0xffffu;
        CTL[C_TK + (x & 7)] = ((u32)(x + 1) << 16) | t;
        if (t >= (u32)NDT) break;
      }
    }
    if (ptask && (P_DLATE >= 2 || (P_DLATE && hw == 1))) {
      u32 en = 0, sp = 0, lo = L_SC;
      if (hw == 1 || !P_OEARLY) while ((en = __builtin_amdgcn_readfirstlane(CTL[C_ISS])) < 0x40000000u && ++sp < SPIN_LDS) __builtin_amdgcn_s_sleep(1);
      if (hw == 1) {
        const int qa = ((int)(en - 0x40000000u) + 1) & ~1;
        sp = 0; while (((int)CTL[C_FREE + (qa & 7)] < qa - 7 || (int)CTL[C_FREE + (qa & 7) + 1] < qa - 6) && ++sp < SPIN_LDS) __builtin_amdgcn_s_sleep(1);
        if (P_DLATE == 3) { sp = 0; while ((int)CTL[C_PUB] < 2 * (int)(en - 0x40000000u) && ++sp < SPIN_LDS) __builtin_amdgcn_s_sleep(1); }
        lo = (u32)(qa & 7) * SLOTB;
      } else { sp = 0; while (CTL[C_CNTV] < (u32)NCONS && ++sp < SPIN_LDS) __builtin_amdgcn_s_sleep(1); }
      stage(lo, 0, 8); frags(wlo, lo); frags(whi, lo + 32 * 384);
      tsh = RT();
    }
    if (hw == 2 && b < 192) {
      const int rg = b & 63;
      u32 sp = 0;
      if (P_PSTART == 0) { while (CTL[C_ISS] < 0x40000000u && ++sp < SPIN_LDS) __builtin_amdgcn_s_sleep(2); }
      else { while (CTL[C_CNTM] < (u32)(P_PSTART == 1 ? NCONS : NCONS - 1) && ++sp < SPIN_LDS) __builtin_amdgcn_s_sleep(1); }
      const u32 off = WS_P + (u32)((rg >> 1) * 16 + (lane >> 3)) * P_STRIDE + (rg & 1) * 512 + (lane & 7) * 16;
      u32x4 d[8], ml;
      bool ok0 = false, ok1 = false, okm = false;
      for (sp = 0;;) {
        if (!okm) ml = g_ld(rws, WS_ML + (u32)((rg >> 1) * 16 + (lane & 15)) * 16);
        if (!ok0) {
#pragma unroll
          for (int i = 0; i < 4; ++i) d[i] = g_ld(rws, off + i * 128);
        }
        if (!ok1) {
#pragma unroll
          for (int i = 0; i < 4; ++i) d[4 + i] = g_ld(rws, off + 8 * P_STRIDE + i * 128);
        }
        okm = tg_ok(ml, tag);
        ok0 = tg_ok(d[0], tag) && tg_ok(d[1], tag) && tg_ok(d[2], tag) && tg_ok(d[3], tag);
        ok1 = tg_ok(d[4], tag) && tg_ok(d[5], tag) && tg_ok(d[6], tag) && tg_ok(d[7], tag);
        if (__all(okm && ok0 && ok1) || ++sp >= SPIN_GLB) break;
        __builtin_amdgcn_s_sleep(P_PSLEEP);
      }
      tsg = RT();
      const float mj = asf(ml[0]);
      const float wj = __builtin_amdgcn_exp2f(mj - row16_max(mj));
      const float sc = wj * __builtin_amdgcn_rcpf(row16_sum(wj * asf(ml[2])));
      u32x4 of;
      const float s0 = asf(__builtin_amdgcn_ds_bpermute((lane >> 3) * 4, asu(sc))), s1 = asf(__builtin_amdgcn_ds_bpermute((lane >> 3) * 4 + 32, asu(sc)));
#pragma unroll
      for (int i = 0; i < 4; ++i) {
        const u32 o = pk2(oct_sum(fmaf(s0, asf(d[i][0]), s1 * asf(d[4 + i][0]))), oct_sum(fmaf(s0, asf(d[i][2]), s1 * asf(d[4 + i][2]))));
        of[i] = o;
      }
      if (lane < 8) *(LAS u32x4*)(L8 + L_OS + lane * 16) = of;
      lds_wait();
      CTL[C_ORDY] = 1;
    }
    if (hw == 0 && ptask) {
      u32 sp = 0; while (CTL[C_ORDY] == 0 && ++sp < SPIN_LDS) {}
      asm volatile("" ::: "memory");
      af[0] = *(LAS u32x4*)(L8 + L_OS + (lane >> 4) * 16); af[1] = *(LAS u32x4*)(L8 + L_OS + 64 + (lane >> 4) * 16);
    }
    if (ptask) {
      float y[12];
#pragma unroll
      for (int t = 0; t < 12; ++t) {
        f32x4 acc = {0.f, 0.f, 0.f, 0.f};
#pragma unroll
        for (int ks = 0; ks < 2; ++ks)
          acc = __builtin_amdgcn_mfma_f32_16x16x32_bf16(__builtin_bit_cast(bf16x8, af[ks]), __builtin_bit_cast(bf16x8, __builtin_shufflevector(wlo[t][ks], whi[t][ks], 0, 1, 2, 3, 4, 5, 6, 7)), acc, 0, 0, 0);
        y[t] = acc[0];
      }
      const int G = lane >> 4;
      const bool odd = lane & 1;
      const float y0 = y[0], y1 = y[1], y2 = y[2], y3 = y[3], y4 = y[4], y5 = y[5], y6 = y[6], y7 = y[7], y8 = y[8], y9 = y[9], y10 = y[10], y11 = y[11];
      const float ya = (G & 2) ? ((G & 1) ? y6 : y4) : ((G & 1) ? y2 : y0), yb = (G & 2) ? ((G & 1) ? y7 : y5) : ((G & 1) ? y3 : y1);
      const float yc = (G & 1) ? y10 : y8, yd = (G & 1) ? y11 : y9;
      const float yan = dpp<XOR1>(ya), ybn = dpp<XOR1>(yb), ycn = dpp<XOR1>(yc), ydn = dpp<XOR1>(yd);
      const u32 base = (hw == 0 ? WS_Y : WS_F) + (u32)(rg * DM + 192 * cg + 32 * G + (odd ? 16 : 0) + (lane & 14)) * 8;
      g_st(rws, base, asu(odd ? ybn : ya), asu(odd ? yb : yan), tag);
      if (lane < 32) g_st(rws, base + 128 * 8, asu(odd ? ydn : yc), asu(odd ? yd : ycn), tag);
      tsp = RT();
    }
    if (hw == 0) { STAMPV(7, tsq); STAMPV(9, tsh); STAMPV(12, tsp); }
    if (hw == 2) STAMPV(11, tsg);
  } else {
    const int ci = wave - 4;
    const int n = lane & 15, G = lane >> 4;
    u32 E = 0;
    if (ci == 0) E = __hip_atomic_load((GAS u32*)(p.ws + WS_E), __ATOMIC_RELAXED, __HIP_MEMORY_SCOPE_AGENT);
    __builtin_amdgcn_sched_barrier(0);
    wg_barrier();
    u64 tsr = 0;
    if (ci == 0) {
      const u32 tg = __builtin_amdgcn_readfirstlane(E) + 1u;
      u32x4 qv[4][2]; u32 sp = 0;
      for (bool ok = false;;) {
        if (!ok) {
#pragma unroll
          for (int k = 0; k < 4; ++k)
#pragma unroll
            for (int j = 0; j < 2; ++j) qv[k][j] = g_ld(rws, WS_Q + (u32)(k * NQ + g * 256 + j * 128 + 2 * lane) * 8);
          ok = true;
#pragma unroll
          for (int k = 0; k < 4; ++k)
#pragma unroll
            for (int j = 0; j < 2; ++j) ok = ok && tg_ok(qv[k][j], tg);
        }
        if (__all(ok) || ++sp >= SPIN_GLB) break;
        __builtin_amdgcn_s_sleep(2);
      }
#pragma unroll
      for (int j = 0; j < 2; ++j) {
        const float q0 = (asf(qv[0][j][0]) + asf(qv[1][j][0])) + (asf(qv[2][j][0]) + asf(qv[3][j][0]));
        const float q1 = (asf(qv[0][j][2]) + asf(qv[1][j][2])) + (asf(qv[2][j][2]) + asf(qv[3][j][2]));
        *(LAS u32*)(L8 + L_QS + j * 256 + lane * 4) = pk2(q0, q1);
      }
      lds_wait();
      CTL[C_QRDY] = 1;
      tsr = RT();
    } else {
      u32 sp = 0; while (CTL[C_QRDY] == 0 && ++sp < SPIN_LDS) __builtin_amdgcn_s_sleep(1);
    }
    asm volatile("" ::: "memory");
    auto wait_pub = [&](int h) {
      u32 sp = 0;
      while ((int)CTL[C_PUB] < h && ++sp < SPIN_LDS) __builtin_amdgcn_s_sleep(1);
      asm volatile("" ::: "memory");
    };
    LAS unsigned char* SC = L8 + L_SC + ci * (NS / NCONS) * 256;
    const u32 tag = CTL[C_TAG];
    u32 ka[4], va[8];
#pragma unroll
    for (int i = 0; i < 4; ++i) ka[i] = n * 256 + (((4 * i + G) ^ n) << 4);
    float m, l;
    {
      bf16x8 qf[4][4];
#pragma unroll
      for (int i = 0; i < 4; ++i) {
        const u32x4 t4 = *(LAS u32x4*)(L8 + L_QS + (n & 1) * 256 + i * 64 + G * 16);
#pragma unroll
        for (int t = 0; t < 4; ++t) qf[t][i] = __builtin_bit_cast(bf16x8, (n >> 1) == t ? t4 : u32x4{0u, 0u, 0u, 0u});
      }
      const u32 sw = ((n >> 2) & 1) * 128 + (n & 1) * 64 + G * 16 + ((n >> 1) & 1) * 8;
      float mx = -INFINITY;
#pragma unroll 1
      for (int it = 0; it < NS / NCONS; ++it) {
        const int q = it * NCONS + ci, s = q & (NSLOT - 1);
        wait_pub(2 * q + 2);
        LAS unsigned char* sb = L8 + s * SLOTB;
        bf16x8 kf[4][4];
#pragma unroll
        for (int t = 0; t < 4; ++t)
#pragma unroll
          for (int i = 0; i < 4; ++i) kf[t][i] = *(LAS bf16x8*)(sb + ka[i] + t * 4096);
        f32x4 sa = {0.f, 0.f, 0.f, 0.f}, sb2 = {0.f, 0.f, 0.f, 0.f};
#pragma unroll
        for (int t = 0; t < 2; ++t)
#pragma unroll
          for (int i = 0; i < 4; ++i) sa = __builtin_amdgcn_mfma_f32_16x16x32_bf16(kf[t][i], qf[t][i], sa, 0, 0, 0);
        asm volatile("s_waitcnt lgkmcnt(0)" : "+v"(kf[2][0]), "+v"(kf[2][1]), "+v"(kf[2][2]), "+v"(kf[2][3]), "+v"(kf[3][0]), "+v"(kf[3][1]), "+v"(kf[3][2]), "+v"(kf[3][3]) :: "memory");
        CTL[C_FREE + s] = q + 1;
#pragma unroll
        for (int t = 2; t < 4; ++t)
#pragma unroll
          for (int i = 0; i < 4; ++i) sb2 = __builtin_amdgcn_mfma_f32_16x16x32_bf16(kf[t][i], qf[t][i], sb2, 0, 0, 0);
        const f32x4 sc = sa + sb2;
        const u32 a0 = pk2(sc[0], sc[1]), a1 = pk2(sc[2], sc[3]);
        const u32 c0 = pk2(bflo(a0) * 0.08838834764831845f, bfhi(a0) * 0.08838834764831845f), c1 = pk2(bflo(a1) * 0.08838834764831845f, bfhi(a1) * 0.08838834764831845f);
        mx = fmaxf(fmaxf(mx, fmaxf(bflo(c0), bfhi(c0))), fmaxf(bflo(c1), bfhi(c1)));
        if (n < 8) *(LAS u32x2*)(SC + it * 256 + sw) = u32x2{c0, c1};
      }
      mx = n < 8 ? mx : -INFINITY;
      mx = fmaxf(mx, dpp<XOR2>(mx)); mx = fmaxf(mx, dpp<ROR4>(mx)); mx = fmaxf(mx, dpp<ROR8>(mx)); mx = x32_max(x16_max(mx));
      m = ceilf(mx * 1.4426950408889634f);
      const float mo = dpp<XOR1>(m), mh = (((lane >> 2) ^ lane) & 1) ? mo : m;
      lds_wait();
      float ls = 0.f;
#pragma unroll
      for (int i = 0; i < (NS / NCONS + 3) / 4; ++i) {
        if ((NS / NCONS) % 4 != 0 && lane + 64 * i >= NS / NCONS * 16) break;
        LAS u32x4* cp = (LAS u32x4*)(SC + (lane + 64 * i) * 16);
        u32x4 z = *cp;
#pragma unroll
        for (int k = 0; k < 4; ++k) {
          const float p0 = __builtin_amdgcn_exp2f(fmaf(bflo(z[k]), 1.4426950408889634f, -mh)), p1 = __builtin_amdgcn_exp2f(fmaf(bfhi(z[k]), 1.4426950408889634f, -mh));
          ls += p0 + p1; z[k] = pk2(p0, p1);
        }
        *cp = z;
      }
      ls += dpp<XOR1>(ls); ls += dpp<XOR2>(ls); ls += dpp<ROR8>(ls); ls = x32_sum(x16_sum(ls));
      const float lo_ = dpp<ROR4>(ls);
      l = (((lane >> 2) ^ lane) & 1) ? lo_ : ls;
      lds_wait();
    }
    f32x4 acc[8];
    {
      const u32 row = 4 * G + (n >> 2), pp = n & 3;
#pragma unroll
      for (int u = 0; u < 8; ++u) { va[u] = row * 256 + ((((2 * u + (pp >> 1)) ^ ((row & 7) << 1))) << 4) + (pp & 1) * 8; acc[u] = f32x4{0.f, 0.f, 0.f, 0.f}; }
#pragma unroll 1
      for (int it = 0; it < NS / NCONS; ++it) {
        const int q = NS + it * NCONS + ci, s = q & (NSLOT - 1);
        const bf16x8 pf0 = *(LAS bf16x8*)(SC + it * 256 + (n & 1) * 64 + G * 16), pf1 = *(LAS bf16x8*)(SC + it * 256 + 128 + (n & 1) * 64 + G * 16);
        wait_pub(2 * q + 2);
        LAS unsigned char* sb = L8 + s * SLOTB;
        s16x4 vf[2][8][2];
#pragma unroll
        for (int h = 0; h < 2; ++h)
#pragma unroll
          for (int u = 0; u < 8; ++u)
#pragma unroll
            for (int t = 0; t < 2; ++t) vf[h][u][t] = __builtin_amdgcn_ds_read_tr16_b64_v4i16((LAS s16x4*)(sb + va[u] + (2 * h + t) * 4096));
#pragma unroll
        for (int u = 0; u < 8; ++u) acc[u] = __builtin_amdgcn_mfma_f32_16x16x32_bf16(__builtin_bit_cast(bf16x8, __builtin_shufflevector(vf[0][u][0], vf[0][u][1], 0, 1, 2, 3, 4, 5, 6, 7)), pf0, acc[u], 0, 0, 0);
        asm volatile("s_waitcnt lgkmcnt(0)" : "+v"(vf[1][0][0]), "+v"(vf[1][0][1]), "+v"(vf[1][1][0]), "+v"(vf[1][1][1]), "+v"(vf[1][2][0]), "+v"(vf[1][2][1]), "+v"(vf[1][3][0]), "+v"(vf[1][3][1]),
                     "+v"(vf[1][4][0]), "+v"(vf[1][4][1]), "+v"(vf[1][5][0]), "+v"(vf[1][5][1]), "+v"(vf[1][6][0]), "+v"(vf[1][6][1]), "+v"(vf[1][7][0]), "+v"(vf[1][7][1]) :: "memory");
        CTL[C_FREE + s] = q + 1;
#pragma unroll
        for (int u = 0; u < 8; ++u) acc[u] = __builtin_amdgcn_mfma_f32_16x16x32_bf16(__builtin_bit_cast(bf16x8, __builtin_shufflevector(vf[1][u][0], vf[1][u][1], 0, 1, 2, 3, 4, 5, 6, 7)), pf1, acc[u], 0, 0, 0);
      }
    }
    lds_arrive(&CTL[C_CNTV], lane);
    {
      bf16x8 qf[4];
#pragma unroll
      for (int i = 0; i < 4; ++i) qf[i] = *(LAS bf16x8*)(L8 + L_QS + (n & 1) * 256 + i * 64 + G * 16);
      float ld = 0.f;
#pragma unroll 1
      for (int q = Q0 + ci;; q += NCONS) {
        const int s = q & (NSLOT - 1);
        bool go;
        for (u32 sp = 0;;) {
          const u32 en = __builtin_amdgcn_readfirstlane(CTL[C_ISS]);
          go = (int)CTL[C_PUB] >= 2 * q + 1;
          if (go || (en >= 0x40000000u && q >= (int)(en - 0x40000000u)) || ++sp >= SPIN_LDS) break;
          __builtin_amdgcn_s_sleep(1);
        }
        if (!go) break;
        asm volatile("" ::: "memory");
        LAS unsigned char* sb = L8 + s * SLOTB;
        f32x4 sc[2];
#pragma unroll
        for (int t = 0; t < 2; ++t) {
          sc[t] = f32x4{0.f, 0.f, 0.f, 0.f};
#pragma unroll
          for (int i = 0; i < 4; ++i) sc[t] = __builtin_amdgcn_mfma_f32_16x16x32_bf16(*(LAS bf16x8*)(sb + ka[i] + t * 4096), qf[i], sc[t], 0, 0, 0);
        }
        float z[8];
#pragma unroll
        for (int t = 0; t < 2; ++t)
#pragma unroll
          for (int k = 0; k < 2; ++k) {
            const u32 a = pk2(sc[t][2 * k], sc[t][2 * k + 1]);
            const u32 c = pk2(bflo(a) * 0.08838834764831845f, bfhi(a) * 0.08838834764831845f);
            z[4 * t + 2 * k] = bflo(c) * 1.4426950408889634f; z[4 * t + 2 * k + 1] = bfhi(c) * 1.4426950408889634f;
          }
        const float zm = fmaxf(fmaxf(fmaxf(z[0], z[1]), fmaxf(z[2], z[3])), fmaxf(fmaxf(z[4], z[5]), fmaxf(z[6], z[7])));
        if (__any(zm > m)) {
          const float mn = fmaxf(m, ceilf(x32_max(x16_max(zm))));
          const float al = __builtin_amdgcn_exp2f(m - mn);
          l *= al; ld *= al; m = mn;
#pragma unroll
          for (int u = 0; u < 8; ++u) acc[u] *= al;
        }
        u32x4 pw;
#pragma unroll
        for (int k = 0; k < 4; ++k) { const float p0 = __builtin_amdgcn_exp2f(z[2 * k] - m), p1 = __builtin_amdgcn_exp2f(z[2 * k + 1] - m); ld += p0 + p1; pw[k] = pk2(p0, p1); }
        asm volatile("" : "+v"(pw));
        wait_pub(2 * q + 2);
        s16x4 vf[8][2];
#pragma unroll
        for (int u = 0; u < 8; ++u)
#pragma unroll
          for (int t = 0; t < 2; ++t) vf[u][t] = __builtin_amdgcn_ds_read_tr16_b64_v4i16((LAS s16x4*)(sb + va[u] + (2 + t) * 4096));
#pragma unroll
        for (int u = 0; u < 8; ++u) acc[u] = __builtin_amdgcn_mfma_f32_16x16x32_bf16(__builtin_bit_cast(bf16x8, __builtin_shufflevector(vf[u][0], vf[u][1], 0, 1, 2, 3, 4, 5, 6, 7)), __builtin_bit_cast(bf16x8, pw), acc[u], 0, 0, 0);
        lds_wait();
        CTL[C_FREE + s] = q + 1;
      }
      l += x32_sum(x16_sum(ld));
    }
    LAS float* MRG = (LAS float*)(L8 + L_MRG);
    if (n < 2) {
      LAS float* MR = MRG + (ci * 2 + n) * 132;
#pragma unroll
      for (int u = 0; u < 8; ++u) *(LAS f32x4*)(MR + 16 * u + 4 * G) = acc[u];
      if (G == 0) { MR[128] = m; MR[129] = l; }
    }
    if (lds_arrive(&CTL[C_CNTM], lane) == NCONS - 1) {
      const int hn = lane >> 5, e = lane & 31;
      const LAS float* MR = MRG + hn * 132;
      float mm[4], M = -INFINITY;
#pragma unroll
      for (int c = 0; c < 4; ++c) { mm[c] = MR[c * 264 + 128]; M = fmaxf(M, mm[c]); }
      f32x2 o0 = {0.f, 0.f}, o1 = {0.f, 0.f}; float L = 0.f;
#pragma unroll
      for (int c = 0; c < 4; ++c) {
        const float w = __builtin_amdgcn_exp2f(mm[c] - M);
        o0 += w * *(const LAS f32x2*)(MR + c * 264 + 2 * e); o1 += w * *(const LAS f32x2*)(MR + c * 264 + 64 + 2 * e); L = fmaf(w, MR[c * 264 + 129], L);
      }
      const u32 base = WS_P + (u32)((2 * g + hn) * 16 + wi) * P_STRIDE;
      g_st(rws, base + e * 16, asu(o0[0]), asu(o0[1]), tag);
      g_st(rws, base + 512 + e * 16, asu(o1[0]), asu(o1[1]), tag);
      if (e == 0) g_st(rws, WS_ML + (u32)((2 * g + hn) * 16 + wi) * 16, asu(M), asu(L), tag);
      STAMPV(10, RT());
    }
    if (ci == 0) STAMPV(8, tsr);
  }
}
}

extern "C" void kernel_launch(void* const* d_in, const int* in_sizes, int n_in, void* d_out, int out_size, void* d_ws, size_t ws_size, hipStream_t stream) {
  fk::Params p;
  p.x = (const u16*)d_in[0]; p.k = (const u16*)d_in[1]; p.v = (const u16*)d_in[2]; p.wq = (const u16*)d_in[3]; p.wo = (const u16*)d_in[4];
  p.wg = (const u16*)d_in[6]; p.wu = (const u16*)d_in[7]; p.wd = (const u16*)d_in[8];
  p.out = (u16*)d_out; p.ws = (unsigned char*)d_ws;
  hipLaunchKernelGGL(fk::fused_decode, dim3(fk::NWG), dim3(fk::NTHR), 0, stream, p);
}
```

```cpp
#include <hip/hip_runtime.h>
#include <hip/hip_bf16.h>
#include <stdint.h>

#define LAS __attribute__((address_space(3)))
#define GAS __attribute__((address_space(1)))
typedef unsigned u32;
typedef unsigned long long u64;
typedef unsigned short u16;
typedef unsigned u32x4 __attribute__((ext_vector_type(4)));
typedef unsigned u32x2 __attribute__((ext_vector_type(2)));
typedef float f32x4 __attribute__((ext_vector_type(4)));
typedef float f32x2 __attribute__((ext_vector_type(2)));
typedef __bf16 bf16x2 __attribute__((ext_vector_type(2)));
typedef __bf16 bf16x8 __attribute__((ext_vector_type(8)));
typedef short s16x4 __attribute__((ext_vector_type(4)));

#define P_WN 22
#define P_DELAYF 0
#define P_NS 56
#define P_DLATE 2
#define P_OEARLY 1
#define P_KP0 0
#define P_KP1 0
#define P_VP0 0
#define P_VP1 0
#define P_WNT 1
#define P_FSTART 1
#define P_FDELAY 16
#define P_PDELAY 10
#define P_FSLEEP 2
#define P_EXP 1
#define P_CODEPF 24000
#define P_PRE 1
#define P_QSTART 1
#define P_QDELAY 8
#define P_QSLEEP 2
#define P_WARM 1
#define P_WAT 24
#define P_PSTART 1
#define P_PSLEEP 1
#define P_LEAD 6

namespace fk {
constexpr int DM = 576, SK = 65536, DH = 128, FF = 4608, NQ = 4096;
constexpr int NWG = 256, NTHR = 512;
constexpr int WN = P_WN, NSLOT = 8, SLOTB = 16384, NCONS = 4;
constexpr int NS = P_NS;
constexpr int Q0 = 2 * NS;
constexpr int NDT = (64 - NS) * 32;
constexpr int NSCH = 3, LEAD = P_LEAD;
static_assert(WN > 16 && WN <= 39 && NS % 4 == 0 && NS >= 8 && NS < 64 && LEAD >= 5 && LEAD <= 10, "knobs (LEAD >= 5: the stream ends only after all three helpers have answered a ticket request)");
constexpr u32 WS_E = 0, WS_CNT = 4096, WS_Q = 8192;
constexpr u32 WS_G = WS_Q + 4 * NQ * 8, WS_U = WS_G + 4 * FF * 8;
constexpr u32 WS_P = WS_U + 4 * FF * 8;
constexpr u32 P_STRIDE = 128 * 8;
constexpr u32 WS_ML = WS_P + 32 * 16 * P_STRIDE;
constexpr u32 WS_Y = WS_ML + 32 * 16 * 16, WS_F = WS_Y + 64 * DM * 8, WS_END = WS_F + 72 * DM * 8;
constexpr u32 L_CTL = NSLOT * SLOTB;
constexpr u32 L_QS = L_CTL + 256;
constexpr u32 L_OS = L_QS + 512;
constexpr u32 L_RS = L_OS + 128;
constexpr u32 L_MRG = L_RS;
constexpr u32 L_SC = L_MRG + 4 * 2 * 132 * 4;
constexpr u32 L_STG = L_SC + NS * 256;
constexpr u32 L_END = L_SC + (P_DLATE < 2 ? NS * 256 + 32 * 384 : 64 * 384);
static_assert(L_END <= 163840 && L_END > 81920 && L_SC % 16 == 0, "LDS");
enum { C_PUB = 0, C_QRDY = 2, C_TAG = 3, C_CNTQ = 4, C_CNTF0 = 5, C_CNTF1 = 6, C_WRDY = 7, C_TK = 8, C_FREE = 16, C_ISS = 32, C_CNTV = 33, C_ORDY = 34, C_FOUT = 35, C_CNTM = 42 };
constexpr u32 SPIN_LDS = 1u << 22, SPIN_GLB = 1u << 19;

struct Params {
  const u16 *x, *k, *v, *wq, *wo, *wg, *wu, *wd;
  u16* out;
  unsigned char* ws;
};

__device__ __forceinline__ float asf(u32 u) { return __uint_as_float(u); }
__device__ __forceinline__ u32 asu(float f) { return __float_as_uint(f); }
__device__ __forceinline__ float bflo(u32 u) { return asf(u << 16); }
__device__ __forceinline__ float bfhi(u32 u) { return asf(u & 0xffff0000u); }
__device__ __forceinline__ u32 pk2(float lo, float hi) { f32x2 f = {lo, hi}; return __builtin_bit_cast(u32, __builtin_convertvector(f, bf16x2)); }
__device__ __forceinline__ float rb(float f) { return bflo(pk2(f, 0.f) & 0xffffu); }
template <int CTRL> __device__ __forceinline__ float dpp(float x) {
  return __builtin_bit_cast(float, __builtin_amdgcn_mov_dpp(__builtin_bit_cast(int, x), CTRL, 0xf, 0xf, true));
}
constexpr int XOR1 = 0xB1, XOR2 = 0x4E, ROR4 = 0x124, ROR8 = 0x128;
__device__ __forceinline__ float x16_sum(float x) { auto s = __builtin_amdgcn_permlane16_swap(asu(x), asu(x), false, false); return asf(s[0]) + asf(s[1]); }
__device__ __forceinline__ float x32_sum(float x) { auto s = __builtin_amdgcn_permlane32_swap(asu(x), asu(x), false, false); return asf(s[0]) + asf(s[1]); }
__device__ __forceinline__ float x16_max(float x) { auto s = __builtin_amdgcn_permlane16_swap(asu(x), asu(x), false, false); return fmaxf(asf(s[0]), asf(s[1])); }
__device__ __forceinline__ float x32_max(float x) { auto s = __builtin_amdgcn_permlane32_swap(asu(x), asu(x), false, false); return fmaxf(asf(s[0]), asf(s[1])); }
__device__ __forceinline__ float row16_sum(float x) { x += dpp<XOR1>(x); x += dpp<XOR2>(x); x += dpp<ROR4>(x); x += dpp<ROR8>(x); return x; }
__device__ __forceinline__ float row16_max(float x) { x = fmaxf(x, dpp<XOR1>(x)); x = fmaxf(x, dpp<XOR2>(x)); x = fmaxf(x, dpp<ROR4>(x)); x = fmaxf(x, dpp<ROR8>(x)); return x; }
__device__ __forceinline__ float wave_sum(float x) { return x32_sum(x16_sum(row16_sum(x))); }
__device__ __forceinline__ float oct_sum(float x) { x += dpp<ROR8>(x); return x32_sum(x16_sum(x)); }
__device__ __forceinline__ float quad_sum(float x) { x += dpp<ROR4>(x); x += dpp<ROR8>(x); return x32_sum(x16_sum(x)); }
__device__ __forceinline__ float sel(bool c, float a, float b) { return c ? a : b; }
__device__ __forceinline__ void lds_wait() { asm volatile("s_waitcnt lgkmcnt(0)" ::: "memory"); }
__device__ __forceinline__ void vm_drain() { asm volatile("s_waitcnt vmcnt(0)" ::: "memory"); }
__device__ __forceinline__ void wg_barrier() { asm volatile("s_waitcnt lgkmcnt(0)\n\ts_barrier" ::: "memory"); }
__device__ __forceinline__ u32x4 ldnt(const u16* p) { return P_WNT ? __builtin_nontemporal_load((const GAS u32x4*)p) : *(const GAS u32x4*)p; }
__device__ __forceinline__ u32x4 mk_srd(const void* p, u32 bytes) {
  const u64 a = (u64)p;
  u32x4 r; r.x = __builtin_amdgcn_readfirstlane((u32)a); r.y = __builtin_amdgcn_readfirstlane((u32)(a >> 32) & 0xffffu); r.z = bytes; r.w = 0x00020000u; return r;
}
__device__ __forceinline__ u32x4 g_ld(__amdgpu_buffer_rsrc_t r, u32 off) { asm volatile("" ::: "memory"); return __builtin_amdgcn_raw_buffer_load_b128(r, off, 0, 16); }
__device__ __forceinline__ void g_st(__amdgpu_buffer_rsrc_t r, u32 off, u32 a, u32 b, u32 tag) { u32x4 v = {a, tag, b, tag}; __builtin_amdgcn_raw_buffer_store_b128(v, r, off, 0, 16); }
__device__ __forceinline__ bool tg_ok(u32x4 v, u32 tag) { return v[1] == tag && v[3] == tag; }
constexpr u32 OOB = 0x80000000u;
template <int N, int SLEEP>
__device__ __forceinline__ void gather(__amdgpu_buffer_rsrc_t r, u32 (&off)[N], u32x4 (&v)[N], u32 tag) {
  for (u32 sp = 0;; ++sp) {
    u32x4 a[N]; u32 all = OOB;
#pragma unroll
    for (int i = 0; i < N; ++i) a[i] = g_ld(r, off[i]);
#pragma unroll
    for (int i = 0; i < N; ++i) { const bool ok = tg_ok(a[i], tag) && off[i] != OOB; v[i][0] = ok ? a[i][0] : v[i][0]; v[i][2] = ok ? a[i][2] : v[i][2]; off[i] = ok ? OOB : off[i]; all &= off[i]; }
    if (__all(all == OOB) || sp >= SPIN_GLB) break;
    __builtin_amdgcn_s_sleep(SLEEP);
  }
}
__device__ __forceinline__ u32 lds_arrive(volatile LAS u32* w, int lane) {
  lds_wait();
  u32 old = 0; if (lane == 0) old = __hip_atomic_fetch_add((LAS u32*)w, 1u, __ATOMIC_RELAXED, __HIP_MEMORY_SCOPE_WORKGROUP);
  old = __builtin_amdgcn_readfirstlane(old);
  asm volatile("" ::: "memory");
  return old;
}

#define DMA4_BODY(POL) \
  asm volatile("s_nop 4\n\ts_mov_b32 m0, %0\n\t" \
               "s_waitcnt vmcnt(%c7)\n\t" \
               "buffer_load_dwordx4 %1, %5, %6 offen " POL " lds\n\t" \
               "s_waitcnt vmcnt(%c7)\n\t" \
               "buffer_load_dwordx4 %2, %5, %6 offen offset:1024 " POL " lds\n\t" \
               "s_waitcnt vmcnt(%c7)\n\t" \
               "buffer_load_dwordx4 %3, %5, %6 offen offset:2048 " POL " lds\n\t" \
               "s_waitcnt vmcnt(%c7)\n\t" \
               "buffer_load_dwordx4 %4, %5, %6 offen offset:3072 " POL " lds" \
               :: "s"(ldsbase), "v"(v0), "v"(v1), "v"(v2), "v"(v3), "s"(srd), "s"(soff), "i"(WN - 1) : "memory")
__device__ __forceinline__ void dma4(u32 ldsbase, u32 v0, u32 v1, u32 v2, u32 v3, u32x4 srd, u32 soff, bool plain = false) {
  if (plain) DMA4_BODY(""); else DMA4_BODY("nt");
}
__device__ __forceinline__ void dma1(u32 ldsbase, u32 v0, u32x4 srd, u32 soff) {
  asm volatile("s_nop 4\n\ts_mov_b32 m0, %0\n\ts_nop 0\n\t"
               "buffer_load_dwordx4 %1, %2, %3 offen nt lds"
               :: "s"(ldsbase), "v"(v0), "s"(srd), "s"(soff) : "memory");
}

__device__ __forceinline__ void fma8(float (&acc)[8], u32x4 w, float xv) {
#pragma unroll
  for (int k = 0; k < 4; ++k) { acc[2 * k] = fmaf(xv, bflo(w[k]), acc[2 * k]); acc[2 * k + 1] = fmaf(xv, bfhi(w[k]), acc[2 * k + 1]); }
}

#define STAMPV(i, v) do {} while (0)
#define RT() 0ull

__global__ void __launch_bounds__(NTHR, 2) fused_decode(Params p) {
  const u64 tsK = RT();
  __shared__ __attribute__((aligned(16))) unsigned char lds[L_END];
  LAS unsigned char* L8 = (LAS unsigned char*)lds;
  volatile LAS u32* CTL = (volatile LAS u32*)(L8 + L_CTL);
  const int b = blockIdx.x, tid = threadIdx.x, lane = tid & 63;
  const int wave = __builtin_amdgcn_readfirstlane(tid >> 6);
  u32 cpf0 = 0, cpf1 = 0;
  if (P_CODEPF && wave >= 4) {
    u64 pc; asm volatile("s_getpc_b64 %0" : "=s"(pc));
    const int o = (tid - 256) * 64;
    cpf0 = *(const GAS u32*)((pc & ~63ull) + o);
    cpf1 = *(const GAS u32*)((pc & ~63ull) + (o + 16384 < P_CODEPF ? o + 16384 : o));
    __builtin_amdgcn_sched_barrier(0);
  }
  const int g = b >> 4, wi = b & 15;
  const __amdgpu_buffer_rsrc_t rws = __builtin_amdgcn_make_buffer_rsrc(p.ws, 0, WS_END, 0x00020000);

  if (__builtin_expect(wave == 0, P_EXP)) {
    __builtin_amdgcn_s_setprio(3);
    __builtin_amdgcn_s_waitcnt(0x0F70);
    const u32 r4 = lane >> 4, pc = lane & 15;
    const u32 ldsb = (u32)(u64)L8;
    u64 ts0 = RT(), ts8 = 0, ts32 = 0, ts64 = 0, ts96 = 0, tsS = 0, stF = 0, stT = 0;
    int pub = 0, q = 0;
    auto wait_free = [&](int s, u32 need) {
      const u64 a_ = RT();
      vm_drain(); pub = 2 * q; CTL[C_PUB] = pub;
      u32 sp = 0; while (CTL[C_FREE + s] < need && ++sp < SPIN_LDS) __builtin_amdgcn_s_sleep(1);
      stF += RT() - a_;
    };
    auto publish = [&](int gr) {
      if (((gr * 4 + 4 - WN) % 8 + 8) % 8 < 4) { pub = max(pub, (q * 16 + gr * 4 + 4 - WN) >> 3); CTL[C_PUB] = pub; }
    };
    const u32x4 rk = mk_srd(p.k + (size_t)g * SK * DH, SK * DH * 2), rv = mk_srd(p.v + (size_t)g * SK * DH, SK * DH * 2);
    const u32 vk0 = r4 * 256 + ((pc ^ r4) << 4), vk1 = r4 * 256 + ((pc ^ (4 + r4)) << 4), vk2 = vk0 ^ 128u, vk3 = vk1 ^ 128u;
    const u32 vv0 = r4 * 256 + ((pc ^ (r4 << 1)) << 4), vv1 = r4 * 256 + ((pc ^ ((4 + r4) << 1)) << 4);
#pragma unroll 1
    for (int ph = 0; ph < 2; ++ph) {
      const u32x4 rs = ph ? rv : rk;
      const u32 a0 = ph ? vv0 : vk0, a1 = ph ? vv1 : vk1, a2 = ph ? vv0 : vk2, a3 = ph ? vv1 : vk3;
      const bool pl0 = ph ? (2 * g >= P_VP0 && 2 * g < P_VP1) : (2 * g >= P_KP0 && 2 * g < P_KP1), pl1 = ph ? (2 * g + 1 >= P_VP0 && 2 * g + 1 < P_VP1) : (2 * g + 1 >= P_KP0 && 2 * g + 1 < P_KP1);
#pragma unroll 1
      for (int i = 0; i < NS; ++i) {
        const u32 r = (u32)(wi + i) & 15u;
        const u32 so1 = ((u32)i << 17) | ((r >> 1) << 14) | ((r & 1u) << 12), so2 = so1 ^ 0x3000u;
        const u32 lb = ldsb + (q & (NSLOT - 1)) * SLOTB;
        dma4(lb, a0, a1, a2, a3, rs, so1, pl0);
        publish(0);
        if (q >= Q0 - 8) CTL[C_ISS] = 2 * q;
        dma4(lb + 4096, a0, a1, a2, a3, rs, so2, pl0);
        publish(1);
        if (q == 1) wg_barrier();
        const u32 fr = CTL[C_FREE + ((q + 1) & (NSLOT - 1))];
        dma4(lb + 8192, a0, a1, a2, a3, rs, so1 + SK * DH, pl1);
        publish(2);
        if (q >= Q0 - 8) CTL[C_ISS] = 2 * q + 1;
        const bool full = q >= NSLOT - 1 && (int)__builtin_amdgcn_readfirstlane(fr) < q + 2 - NSLOT;
        dma4(lb + 12288, a0, a1, a2, a3, rs, so2 + SK * DH, pl1);
        publish(3);
        ++q;
        if (__builtin_expect(full, 0)) wait_free(q & (NSLOT - 1), q + 1 - NSLOT);
      }
    }
    tsS = RT();
    {
      int x = Q0, sc = 0, xend = 0x7fffffff, dead = 0;
      u32 e = __builtin_amdgcn_readfirstlane(CTL[C_TK + (x & 7)]);
#pragma unroll 1
      for (;;) {
        if (__builtin_expect((int)(e >> 16) != x + 1 || (e & 0xffffu) >= (u32)NDT, 0)) {
          for (;;) {
            if (!((dead >> sc) & 1)) {
              e = __builtin_amdgcn_readfirstlane(CTL[C_TK + (x & 7)]);
              if ((int)(e >> 16) != x + 1) {
                const u64 a_ = RT();
                vm_drain(); pub = 2 * q; CTL[C_PUB] = pub;
                u32 sp = 0;
                while ((int)((e = __builtin_amdgcn_readfirstlane(CTL[C_TK + (x & 7)])) >> 16) != x + 1 && ++sp < SPIN_LDS) __builtin_amdgcn_s_sleep(1);
                stT += RT() - a_;
                if (sp >= SPIN_LDS) e = 0xffffu;
              }
              if ((e & 0xffffu) < (u32)NDT) break;
              dead |= 1 << sc;
              if (xend == 0x7fffffff) xend = (2 * x - 1 + LEAD) >> 1;
            }
            ++x; sc = sc == NSCH - 1 ? 0 : sc + 1;
            if (x > xend) break;
          }
        }
        if (x > xend) break;
        ++x; sc = sc == NSCH - 1 ? 0 : sc + 1;
        const u32 t = e & 0xffffu, tl = t >> 1;
        const u32 so1 = ((u32)NS << 17) + (((tl >> 1) << 14) | ((tl & 1u) << 12)) + (t & 1u) * (SK * DH), so2 = so1 ^ 0x3000u;
        const u32 lb = ldsb + (q & (NSLOT - 1)) * SLOTB;
        const bool ann = xend == 0x7fffffff;
        dma4(lb, vk0, vk1, vk2, vk3, rk, so1);
        publish(0);
        if (ann) CTL[C_ISS] = 2 * q;
        dma4(lb + 4096, vk0, vk1, vk2, vk3, rk, so2);
        publish(1);
        const u32 fr = CTL[C_FREE + ((q + 1) & (NSLOT - 1))];
        dma4(lb + 8192, vv0, vv1, vv0, vv1, rv, so1);
        publish(2);
        if (ann) CTL[C_ISS] = 2 * q + 1;
        const bool full = (int)__builtin_amdgcn_readfirstlane(fr) < q + 2 - NSLOT;
        const u32 en = CTL[C_TK + (x & 7)];
        dma4(lb + 12288, vv0, vv1, vv0, vv1, rv, so2);
        publish(3);
        e = __builtin_amdgcn_readfirstlane(en);
        ++q;
        if (__builtin_expect(full, 0)) wait_free(q & (NSLOT - 1), q + 1 - NSLOT);
        if (x > xend) break;
      }
    }
    CTL[C_ISS] = 0x40000000u + (u32)q;
    if (WN > 24) { asm volatile("s_waitcnt vmcnt(24)" ::: "memory"); pub = max(pub, 2 * q - 3); CTL[C_PUB] = pub; }
    asm volatile("s_waitcnt vmcnt(16)" ::: "memory"); pub = max(pub, 2 * q - 2); CTL[C_PUB] = pub;
    asm volatile("s_waitcnt vmcnt(8)" ::: "memory"); pub = max(pub, 2 * q - 1); CTL[C_PUB] = pub;
    vm_drain(); CTL[C_PUB] = 2 * q;
    const u64 tsE = RT();
    __builtin_amdgcn_s_setprio(0);
    u64 tsG = 0, tsO = 0;
    if (b < 72) {
      const u32 tag = CTL[C_TAG];
      const int r16 = lane >> 2, e = lane & 3;
      const u32 off = (u32)(r16 * DM + 8 * b + 2 * e) * 8;
      const u32 xx = *(const GAS u32*)(p.x + 8 * b + 2 * e);
      u32 sp = 0;
      while (CTL[C_FOUT] == 0 && ++sp < SPIN_LDS) __builtin_amdgcn_s_sleep(1);
      __builtin_amdgcn_s_sleep(P_FDELAY);
      u32x4 fv[5], yv[4];
      u32 fo[5] = {WS_F + off, WS_F + off + 16 * DM * 8, WS_F + off + 32 * DM * 8, WS_F + off + 48 * DM * 8, r16 < 8 ? WS_F + off + 64 * DM * 8 : OOB};
      u32 yo[4] = {WS_Y + off, WS_Y + off + 16 * DM * 8, WS_Y + off + 32 * DM * 8, WS_Y + off + 48 * DM * 8};
      fv[4] = u32x4{0u, 0u, 0u, 0u};
      gather<5, P_FSLEEP>(rws, fo, fv, tag);
      float gt[2];
#pragma unroll
      for (int k = 0; k < 2; ++k) {
        const float f = rb(quad_sum(((asf(fv[0][2 * k]) + asf(fv[1][2 * k])) + (asf(fv[2][2 * k]) + asf(fv[3][2 * k]))) + asf(fv[4][2 * k])));
        gt[k] = rb(f / (1.f + __expf(-f)));
      }
      ts96 = RT();
      sp = 0;
      if (P_FSTART) while (CTL[C_ORDY] == 0 && ++sp < SPIN_LDS) __builtin_amdgcn_s_sleep(1);
      gather<4, 1>(rws, yo, yv, tag);
      tsG = RT();
      float r[2];
#pragma unroll
      for (int k = 0; k < 2; ++k) {
        const float sv = rb(quad_sum((asf(yv[0][2 * k]) + asf(yv[1][2 * k])) + (asf(yv[2][2 * k]) + asf(yv[3][2 * k]))));
        r[k] = (k ? bfhi(xx) : bflo(xx)) + rb(sv * gt[k]);
      }
      if (lane < 4) *(GAS u32*)(p.out + 8 * b + 2 * e) = pk2(r[0], r[1]);
      if (b == 0 && lane == 0) __hip_atomic_store((GAS u32*)(p.ws + WS_E), tag, __ATOMIC_RELAXED, __HIP_MEMORY_SCOPE_AGENT);
      tsO = RT();
    }
    STAMPV(0, tsK); STAMPV(15, ts0); STAMPV(1, ts8); STAMPV(2, ts32); STAMPV(3, ts64); STAMPV(4, ts96); STAMPV(5, tsS); STAMPV(6, tsE); STAMPV(13, tsG); STAMPV(14, tsO);
    STAMPV(16, stF); STAMPV(18, stT); STAMPV(20, q);
  } else if (wave <= 3) {
    const int hw = wave - 1, tau = tid - 64;
    const int ch = tau & 7, rr = tau >> 3;
    const int kq = b & 3, row0 = kq * 144 + rr;
    const int cq = b >> 2;
    const u32 E = __hip_atomic_load((GAS u32*)(p.ws + WS_E), __ATOMIC_RELAXED, __HIP_MEMORY_SCOPE_AGENT);
    if (hw == 0 && wi == 0 && lane == 0) __hip_atomic_store((GAS u32*)(p.ws + WS_CNT + g * 256), 0u, __ATOMIC_RELAXED, __HIP_MEMORY_SCOPE_AGENT);
    asm volatile("" ::: "memory");
    u16 xs[9], xm[6];
#pragma unroll
    for (int i = 0; i < 9; ++i) xs[i] = p.x[lane + 64 * i];
#pragma unroll
    for (int i = 0; i < 6; ++i) xm[i] = p.x[row0 + 24 * i];
    u32x4 wa_[6], wb_[6];
#pragma unroll
    for (int i = 0; i < 6; ++i) wa_[i] = ldnt(p.wq + (size_t)(row0 + 24 * i) * NQ + cq * 64 + ch * 8);
    if (hw == 0) CTL[lane] = 0;
    __builtin_amdgcn_sched_barrier(0);
    wg_barrier();
    __builtin_amdgcn_sched_barrier(0);
    const u32 tag = __builtin_amdgcn_readfirstlane(E) + 1u;
    if (hw == 0 && lane == 0) CTL[C_TAG] = tag;
    float sum = 0.f;
#pragma unroll
    for (int i = 0; i < 9; ++i) sum += bflo(xs[i]);
    const float mean = wave_sum(sum) * (1.f / DM);
    float var = 0.f;
#pragma unroll
    for (int i = 0; i < 9; ++i) { const float d = bflo(xs[i]) - mean; var = fmaf(d, d, var); }
    var = wave_sum(var) * (1.f / DM);
    const float mb = rb(mean), den = rb(sqrtf(rb(rb(var) + 1e-5f)));
    float xn[6];
#pragma unroll
    for (int i = 0; i < 6; ++i) xn[i] = rb(rb(bflo(xm[i]) - mb) / den);
    LAS float* RS = (LAS float*)(L8 + L_RS);
    u64 tsq = 0, tsh = 0, tsg = 0, tsp = 0;
    {
      float a[8] = {0.f, 0.f, 0.f, 0.f, 0.f, 0.f, 0.f, 0.f};
#pragma unroll
      for (int i = 0; i < 6; ++i) fma8(a, wa_[i], xn[i]);
#pragma unroll
      for (int k = 0; k < 8; ++k) a[k] = oct_sum(a[k]);
      if (lane < 8) { *(LAS f32x4*)(RS + hw * 64 + lane * 8) = f32x4{a[0], a[1], a[2], a[3]}; *(LAS f32x4*)(RS + hw * 64 + lane * 8 + 4) = f32x4{a[4], a[5], a[6], a[7]}; }
      vm_drain();
      if (lds_arrive(&CTL[C_CNTQ], lane) == 2) {
        if (lane < 32) {
          const f32x2 t = *(LAS f32x2*)(RS + lane * 2) + *(LAS f32x2*)(RS + 64 + lane * 2) + *(LAS f32x2*)(RS + 128 + lane * 2);
          g_st(rws, WS_Q + (u32)(kq * NQ + cq * 64 + lane * 2) * 8, asu(t[0]), asu(t[1]), tag);
        }
      }
      tsq = RT();
    }
    if (P_DELAYF) { u32 sp = 0; while (CTL[C_QRDY] == 0 && ++sp < SPIN_LDS) __builtin_amdgcn_s_sleep(4); }
#pragma unroll 1
    for (int pass = 0; pass < (b < 32 ? 2 : 1); ++pass) {
      const int cs = cq + 64 * pass;
#pragma unroll
      for (int i = 0; i < 6; ++i) { wa_[i] = ldnt(p.wg + (size_t)(row0 + 24 * i) * FF + cs * 64 + ch * 8); wb_[i] = ldnt(p.wu + (size_t)(row0 + 24 * i) * FF + cs * 64 + ch * 8); }
      float a[8] = {0.f, 0.f, 0.f, 0.f, 0.f, 0.f, 0.f, 0.f}, c[8] = {0.f, 0.f, 0.f, 0.f, 0.f, 0.f, 0.f, 0.f};
#pragma unroll
      for (int i = 0; i < 6; ++i) { fma8(a, wa_[i], xn[i]); fma8(c, wb_[i], xn[i]); }
#pragma unroll
      for (int k = 0; k < 8; ++k) { a[k] = oct_sum(a[k]); c[k] = oct_sum(c[k]); }
      LAS float* RG = RS + 192 + pass * 384; LAS float* RU = RG + 192;
      if (lane < 8) {
        *(LAS f32x4*)(RG + hw * 64 + lane * 8) = f32x4{a[0], a[1], a[2], a[3]}; *(LAS f32x4*)(RG + hw * 64 + lane * 8 + 4) = f32x4{a[4], a[5], a[6], a[7]};
        *(LAS f32x4*)(RU + hw * 64 + lane * 8) = f32x4{c[0], c[1], c[2], c[3]}; *(LAS f32x4*)(RU + hw * 64 + lane * 8 + 4) = f32x4{c[4], c[5], c[6], c[7]};
      }
      if (lds_arrive(&CTL[C_CNTF0 + pass], lane) == 2) {
        const LAS float* R = (lane < 32 ? RG : RU) + (lane & 31) * 2;
        const f32x2 t = *(const LAS f32x2*)R + *(const LAS f32x2*)(R + 64) + *(const LAS f32x2*)(R + 128);
        g_st(rws, (lane < 32 ? WS_G : WS_U) + (u32)(kq * FF + cs * 64 + (lane & 31) * 2) * 8, asu(t[0]), asu(t[1]), tag);
      }
    }
    tsh = RT();
    const bool ptask = hw == 0 ? b < 192 : (hw == 1 && b < 216);
    const int rg = hw == 0 ? (b & 63) : b % 72, cg = hw == 0 ? b >> 6 : b / 72;
    const int rs = hw == 0 ? 4 : 2, rbase = hw == 0 ? 128 * (rg >> 1) + 2 * (rg & 1) : 64 * rg;
    const u32x4 rw = mk_srd((hw == 0 ? p.wo : p.wd) + (size_t)rbase * DM + 192 * cg, ptask ? ((31 * rs + 1) * DM + 192) * 2 : 0);
    const u32 wa = (16 * ((lane >> 3) & 1) + 2 * (lane >> 4) + ((lane >> 2) & 1)) * 384 + (lane & 3) * 8;
    u32x4 af[2];
    s16x4 wlo[12][2], whi[12][2];
    auto stage = [&](u32 ldsoff, int row0, int nrow8) {
      u32 vo[3];
#pragma unroll
      for (int e = 0; e < 3; ++e) { const u32 u = (u32)(e * 64 + lane), row = u / 24u; vo[e] = ((row >> 1) * rs + (row & 1u)) * (DM * 2) + (u - row * 24u) * 16u; }
#pragma unroll 1
      for (int r = 0; r < nrow8; ++r)
#pragma unroll
        for (int e = 0; e < 3; ++e) dma1((u32)(u64)L8 + ldsoff + r * 3072 + e * 1024, vo[e], rw, (u32)((row0 + 8 * r) / 2 * rs) * (DM * 2));
      vm_drain();
    };
    auto frags = [&](s16x4 (&w)[12][2], u32 ldsoff) {
#pragma unroll
      for (int t = 0; t < 12; ++t)
#pragma unroll
        for (int ks = 0; ks < 2; ++ks) w[t][ks] = __builtin_amdgcn_ds_read_tr16_b64_v4i16((LAS s16x4*)(L8 + ldsoff + wa + (8 * ks) * 384 + t * 32));
    };
    if (ptask) {
      u32 sp = 0; while ((int)CTL[C_PUB] < 2 * P_WAT && ++sp < SPIN_LDS) __builtin_amdgcn_s_sleep(16);
      if (hw == 1) {
        const u32 off = (u32)(64 * rg + 2 * (lane & 31)) * 8;
        u32x4 gv[4], uv[4];
        for (sp = 0;;) {
          bool ok = true;
#pragma unroll
          for (int k = 0; k < 4; ++k) { gv[k] = g_ld(rws, WS_G + k * FF * 8 + off); uv[k] = g_ld(rws, WS_U + k * FF * 8 + off); }
#pragma unroll
          for (int k = 0; k < 4; ++k) ok = ok && tg_ok(gv[k], tag) && tg_ok(uv[k], tag);
          if (__all(ok) || ++sp >= SPIN_GLB) break;
          __builtin_amdgcn_s_sleep(32);
        }
        float hh[2];
#pragma unroll
        for (int k = 0; k < 2; ++k) {
          const float z = rb((asf(gv[0][2 * k]) + asf(gv[1][2 * k])) + (asf(gv[2][2 * k]) + asf(gv[3][2 * k])));
          const float u = rb((asf(uv[0][2 * k]) + asf(uv[1][2 * k])) + (asf(uv[2][2 * k]) + asf(uv[3][2 * k])));
          const float ge = rb(0.5f * z * (1.f + tanhf(0.7978845608028654f * (z + 0.044715f * z * z * z))));
          hh[k] = ge * u;
        }
        const u32 hp = pk2(hh[0], hh[1]);
#pragma unroll
        for (int ks = 0; ks < 2; ++ks)
#pragma unroll
          for (int i = 0; i < 4; ++i) af[ks][i] = __builtin_amdgcn_ds_bpermute((8 * i + 4 * ks + (lane >> 4)) * 4, hp);
        if (!P_DLATE && b < 192) { sp = 0; while (CTL[C_WRDY] == 0 && ++sp < SPIN_LDS) __builtin_amdgcn_s_sleep(8); }
      }
      if ((hw == 0 && P_DLATE < 2) || !P_DLATE) {
        stage(L_STG, 0, 4); frags(wlo, L_STG); lds_wait();
        stage(L_STG, 32, 4); frags(whi, L_STG); lds_wait();
        CTL[C_WRDY] = 1;
      }
    }
    {
      u32 sp = 0; while (CTL[C_QRDY] == 0 && ++sp < SPIN_LDS) __builtin_amdgcn_s_sleep(4);
      for (int x = Q0 + hw;; x += NSCH) {
        int is; sp = 0;
        while ((is = (int)CTL[C_ISS]) < 2 * x - LEAD && ++sp < SPIN_LDS) { if (is == 0) __builtin_amdgcn_s_sleep(16); else __builtin_amdgcn_s_sleep(1); }
        if (is >= 0x40000000 || sp >= SPIN_LDS) break;
        u32 t = 0;
        if (lane == 0) t = __hip_atomic_fetch_add((GAS u32*)(p.ws + WS_CNT + g * 256), 1u, __ATOMIC_RELAXED, __HIP_MEMORY_SCOPE_AGENT);
        t = __builtin_amdgcn_readfirstlane(t);
        if (t > 0xffffu) t = 0xffffu;
        CTL[C_TK + (x & 7)] = ((u32)(x + 1) << 16) | t;
        if (t >= (u32)NDT) break;
      }
    }
    if (ptask && (P_DLATE >= 2 || (P_DLATE && hw == 1))) {
      u32 en = 0, sp = 0, lo = L_SC;
      if (hw == 1 || !P_OEARLY) while ((en = __builtin_amdgcn_readfirstlane(CTL[C_ISS])) < 0x40000000u && ++sp < SPIN_LDS) __builtin_amdgcn_s_sleep(1);
      if (hw == 1) {
        const int qa = ((int)(en - 0x40000000u) + 1) & ~1;
        sp = 0; while (((int)CTL[C_FREE + (qa & 7)] < qa - 7 || (int)CTL[C_FREE + (qa & 7) + 1] < qa - 6) && ++sp < SPIN_LDS) __builtin_amdgcn_s_sleep(1);
        if (P_DLATE == 3) { sp = 0; while ((int)CTL[C_PUB] < 2 * (int)(en - 0x40000000u) && ++sp < SPIN_LDS) __builtin_amdgcn_s_sleep(1); }
        lo = (u32)(qa & 7) * SLOTB;
      } else { sp = 0; while (CTL[C_CNTV] < (u32)NCONS && ++sp < SPIN_LDS) __builtin_amdgcn_s_sleep(1); }
      stage(lo, 0, 8); frags(wlo, lo); frags(whi, lo + 32 * 384);
      tsh = RT();
    }
    if (hw == 2 && b < 192) {
      const int rg = b & 63;
      u32 sp = 0;
      if (P_PSTART == 0) { while (CTL[C_ISS] < 0x40000000u && ++sp < SPIN_LDS) __builtin_amdgcn_s_sleep(2); }
      else { while (CTL[C_CNTM] < (u32)(P_PSTART == 1 ? NCONS : NCONS - 1) && ++sp < SPIN_LDS) __builtin_amdgcn_s_sleep(1); }
      if (P_PDELAY) __builtin_amdgcn_s_sleep(P_PDELAY);
      const u32 off = WS_P + (u32)((rg >> 1) * 16 + (lane >> 3)) * P_STRIDE + (rg & 1) * 512 + (lane & 7) * 16;
      u32x4 d[8], ml;
      bool ok0 = false, ok1 = false, okm = false;
      for (sp = 0;;) {
        if (!okm) ml = g_ld(rws, WS_ML + (u32)((rg >> 1) * 16 + (lane & 15)) * 16);
        if (!ok0) {
#pragma unroll
          for (int i = 0; i < 4; ++i) d[i] = g_ld(rws, off + i * 128);
        }
        if (!ok1) {
#pragma unroll
          for (int i = 0; i < 4; ++i) d[4 + i] = g_ld(rws, off + 8 * P_STRIDE + i * 128);
        }
        okm = tg_ok(ml, tag);
        ok0 = tg_ok(d[0], tag) && tg_ok(d[1], tag) && tg_ok(d[2], tag) && tg_ok(d[3], tag);
        ok1 = tg_ok(d[4], tag) && tg_ok(d[5], tag) && tg_ok(d[6], tag) && tg_ok(d[7], tag);
        if (__all(okm && ok0 && ok1) || ++sp >= SPIN_GLB) break;
        __builtin_amdgcn_s_sleep(P_PSLEEP);
      }
      tsg = RT();
      const float mj = asf(ml[0]);
      const float wj = __builtin_amdgcn_exp2f(mj - row16_max(mj));
      const float sc = wj * __builtin_amdgcn_rcpf(row16_sum(wj * asf(ml[2])));
      u32x4 of;
      const float s0 = asf(__builtin_amdgcn_ds_bpermute((lane >> 3) * 4, asu(sc))), s1 = asf(__builtin_amdgcn_ds_bpermute((lane >> 3) * 4 + 32, asu(sc)));
#pragma unroll
      for (int i = 0; i < 4; ++i) {
        const u32 o = pk2(oct_sum(fmaf(s0, asf(d[i][0]), s1 * asf(d[4 + i][0]))), oct_sum(fmaf(s0, asf(d[i][2]), s1 * asf(d[4 + i][2]))));
        of[i] = o;
      }
      if (lane < 8) *(LAS u32x4*)(L8 + L_OS + lane * 16) = of;
      lds_wait();
      CTL[C_ORDY] = 1;
    }
    if (hw == 0 && ptask) {
      u32 sp = 0; while (CTL[C_ORDY] == 0 && ++sp < SPIN_LDS) {}
      asm volatile("" ::: "memory");
      af[0] = *(LAS u32x4*)(L8 + L_OS + (lane >> 4) * 16); af[1] = *(LAS u32x4*)(L8 + L_OS + 64 + (lane >> 4) * 16);
    }
    if (ptask) {
      float y[12];
#pragma unroll
      for (int t = 0; t < 12; ++t) {
        f32x4 acc = {0.f, 0.f, 0.f, 0.f};
#pragma unroll
        for (int ks = 0; ks < 2; ++ks)
          acc = __builtin_amdgcn_mfma_f32_16x16x32_bf16(__builtin_bit_cast(bf16x8, af[ks]), __builtin_bit_cast(bf16x8, __builtin_shufflevector(wlo[t][ks], whi[t][ks], 0, 1, 2, 3, 4, 5, 6, 7)), acc, 0, 0, 0);
        y[t] = acc[0];
      }
      const int G = lane >> 4;
      const bool odd = lane & 1;
      const float y0 = y[0], y1 = y[1], y2 = y[2], y3 = y[3], y4 = y[4], y5 = y[5], y6 = y[6], y7 = y[7], y8 = y[8], y9 = y[9], y10 = y[10], y11 = y[11];
      const bool g0 = G & 1, g1 = G & 2;
      const float ya = sel(g1, sel(g0, y6, y4), sel(g0, y2, y0)), yb = sel(g1, sel(g0, y7, y5), sel(g0, y3, y1));
      const float yc = sel(g0, y10, y8), yd = sel(g0, y11, y9);
      const float yan = dpp<XOR1>(ya), ybn = dpp<XOR1>(yb), ycn = dpp<XOR1>(yc), ydn = dpp<XOR1>(yd);
      const u32 base = (hw == 0 ? WS_Y : WS_F) + (u32)(rg * DM + 192 * cg + 32 * G + (odd ? 16 : 0) + (lane & 14)) * 8;
      g_st(rws, base, asu(odd ? ybn : ya), asu(odd ? yb : yan), tag);
      if (lane < 32) g_st(rws, base + 128 * 8, asu(odd ? ydn : yc), asu(odd ? yd : ycn), tag);
      tsp = RT();
      if (hw == 1) CTL[C_FOUT] = 1;
    }
    if (hw == 0) { STAMPV(7, tsq); STAMPV(9, tsh); STAMPV(12, tsp); }
    if (hw == 2) STAMPV(11, tsg);
    if (hw == 1) STAMPV(21, tsp);
  } else {
    const int ci = wave - 4;
    const int n = lane & 15, G = lane >> 4;
    u32 E = 0;
    if (ci == 0) E = __hip_atomic_load((GAS u32*)(p.ws + WS_E), __ATOMIC_RELAXED, __HIP_MEMORY_SCOPE_AGENT);
    __builtin_amdgcn_sched_barrier(0);
    wg_barrier();
    asm volatile("" :: "v"(cpf0), "v"(cpf1));
    u64 tsr = 0;
    auto wait_pub = [&](int h) {
      u32 sp = 0;
      while ((int)CTL[C_PUB] < h && ++sp < SPIN_LDS) __builtin_amdgcn_s_sleep(1);
      asm volatile("" ::: "memory");
    };
    u32 ka[4], va[8];
#pragma unroll
    for (int i = 0; i < 4; ++i) ka[i] = n * 256 + (((4 * i + G) ^ n) << 4);
    bf16x8 kf[4][4];
    auto load_k = [&](int q) {
      wait_pub(2 * q + 2);
      LAS unsigned char* sb = L8 + (q & (NSLOT - 1)) * SLOTB;
#pragma unroll
      for (int t = 0; t < 4; ++t)
#pragma unroll
        for (int i = 0; i < 4; ++i) kf[t][i] = *(LAS bf16x8*)(sb + ka[i] + t * 4096);
      asm volatile("s_waitcnt lgkmcnt(0)" : "+v"(kf[0][0]), "+v"(kf[0][1]), "+v"(kf[0][2]), "+v"(kf[0][3]), "+v"(kf[1][0]), "+v"(kf[1][1]), "+v"(kf[1][2]), "+v"(kf[1][3]),
                   "+v"(kf[2][0]), "+v"(kf[2][1]), "+v"(kf[2][2]), "+v"(kf[2][3]), "+v"(kf[3][0]), "+v"(kf[3][1]), "+v"(kf[3][2]), "+v"(kf[3][3]) :: "memory");
      CTL[C_FREE + (q & (NSLOT - 1))] = q + 1;
    };
    if (P_PRE) load_k(ci);
    if (ci == 0) {
      const u32 tg = __builtin_amdgcn_readfirstlane(E) + 1u;
      if (P_QSTART) { u32 sp = 0; while (CTL[C_CNTQ] < 3 && ++sp < SPIN_LDS) __builtin_amdgcn_s_sleep(2); __builtin_amdgcn_s_sleep(P_QDELAY); }
      u32x4 qv[8]; u32 of[8];
#pragma unroll
      for (int k = 0; k < 8; ++k) of[k] = WS_Q + (u32)((k >> 1) * NQ + g * 256 + (k & 1) * 128 + 2 * lane) * 8;
      gather<8, P_QSLEEP>(rws, of, qv, tg);
#pragma unroll
      for (int j = 0; j < 2; ++j) {
        const float q0 = (asf(qv[j][0]) + asf(qv[2 + j][0])) + (asf(qv[4 + j][0]) + asf(qv[6 + j][0]));
        const float q1 = (asf(qv[j][2]) + asf(qv[2 + j][2])) + (asf(qv[4 + j][2]) + asf(qv[6 + j][2]));
        *(LAS u32*)(L8 + L_QS + j * 256 + lane * 4) = pk2(q0, q1);
      }
      lds_wait();
      CTL[C_QRDY] = 1;
      tsr = RT();
    } else {
      u32 sp = 0; while (CTL[C_QRDY] == 0 && ++sp < SPIN_LDS) __builtin_amdgcn_s_sleep(1);
    }
    asm volatile("" ::: "memory");
    LAS unsigned char* SC = L8 + L_SC + ci * (NS / NCONS) * 256;
    const u32 tag = CTL[C_TAG];
    float m, l;
    {
      bf16x8 qf[4][4];
#pragma unroll
      for (int i = 0; i < 4; ++i) {
        const u32x4 t4 = *(LAS u32x4*)(L8 + L_QS + (n & 1) * 256 + i * 64 + G * 16);
#pragma unroll
        for (int t = 0; t < 4; ++t) qf[t][i] = __builtin_bit_cast(bf16x8, (n >> 1) == t ? t4 : u32x4{0u, 0u, 0u, 0u});
      }
      const u32 sw = ((n >> 2) & 1) * 128 + (n & 1) * 64 + G * 16 + ((n >> 1) & 1) * 8;
      float mx = -INFINITY;
#pragma unroll 1
      for (int it = 0; it < NS / NCONS; ++it) {
        if (!P_PRE || it > 0) load_k(it * NCONS + ci);
        f32x4 sa = {0.f, 0.f, 0.f, 0.f}, sb2 = {0.f, 0.f, 0.f, 0.f};
#pragma unroll
        for (int i = 0; i < 4; ++i) {
          sa = __builtin_amdgcn_mfma_f32_16x16x32_bf16(kf[0][i], qf[0][i], sa, 0, 0, 0); sb2 = __builtin_amdgcn_mfma_f32_16x16x32_bf16(kf[2][i], qf[2][i], sb2, 0, 0, 0);
          sa = __builtin_amdgcn_mfma_f32_16x16x32_bf16(kf[1][i], qf[1][i], sa, 0, 0, 0); sb2 = __builtin_amdgcn_mfma_f32_16x16x32_bf16(kf[3][i], qf[3][i], sb2, 0, 0, 0);
        }
        const f32x4 sc = sa + sb2;
        const u32 a0 = pk2(sc[0], sc[1]), a1 = pk2(sc[2], sc[3]);
        const u32 c0 = pk2(bflo(a0) * 0.08838834764831845f, bfhi(a0) * 0.08838834764831845f), c1 = pk2(bflo(a1) * 0.08838834764831845f, bfhi(a1) * 0.08838834764831845f);
        mx = fmaxf(fmaxf(mx, fmaxf(bflo(c0), bfhi(c0))), fmaxf(bflo(c1), bfhi(c1)));
        if (n < 8) *(LAS u32x2*)(SC + it * 256 + sw) = u32x2{c0, c1};
      }
      mx = n < 8 ? mx : -INFINITY;
      mx = fmaxf(mx, dpp<XOR2>(mx)); mx = fmaxf(mx, dpp<ROR4>(mx)); mx = fmaxf(mx, dpp<ROR8>(mx)); mx = x32_max(x16_max(mx));
      m = ceilf(mx * 1.4426950408889634f);
      const float mo = dpp<XOR1>(m), mh = (((lane >> 2) ^ lane) & 1) ? mo : m;
      lds_wait();
      float ls = 0.f;
#pragma unroll
      for (int i = 0; i < (NS / NCONS + 3) / 4; ++i) {
        if ((NS / NCONS) % 4 != 0 && lane + 64 * i >= NS / NCONS * 16) break;
        LAS u32x4* cp = (LAS u32x4*)(SC + (lane + 64 * i) * 16);
        u32x4 z = *cp;
#pragma unroll
        for (int k = 0; k < 4; ++k) {
          const float p0 = __builtin_amdgcn_exp2f(fmaf(bflo(z[k]), 1.4426950408889634f, -mh)), p1 = __builtin_amdgcn_exp2f(fmaf(bfhi(z[k]), 1.4426950408889634f, -mh));
          ls += p0 + p1; z[k] = pk2(p0, p1);
        }
        *cp = z;
      }
      ls += dpp<XOR1>(ls); ls += dpp<XOR2>(ls); ls += dpp<ROR8>(ls); ls = x32_sum(x16_sum(ls));
      const float lo_ = dpp<ROR4>(ls);
      l = (((lane >> 2) ^ lane) & 1) ? lo_ : ls;
      lds_wait();
    }
    f32x4 acc[8];
    {
      const u32 row = 4 * G + (n >> 2), pp = n & 3;
#pragma unroll
      for (int u = 0; u < 8; ++u) { va[u] = row * 256 + ((((2 * u + (pp >> 1)) ^ ((row & 7) << 1))) << 4) + (pp & 1) * 8; acc[u] = f32x4{0.f, 0.f, 0.f, 0.f}; }
#pragma unroll 1
      for (int it = 0; it < NS / NCONS; ++it) {
        const int q = NS + it * NCONS + ci, s = q & (NSLOT - 1);
        const bf16x8 pf0 = *(LAS bf16x8*)(SC + it * 256 + (n & 1) * 64 + G * 16), pf1 = *(LAS bf16x8*)(SC + it * 256 + 128 + (n & 1) * 64 + G * 16);
        wait_pub(2 * q + 2);
        LAS unsigned char* sb = L8 + s * SLOTB;
        s16x4 vf[2][8][2];
#pragma unroll
        for (int h = 0; h < 2; ++h)
#pragma unroll
          for (int u = 0; u < 8; ++u)
#pragma unroll
            for (int t = 0; t < 2; ++t) vf[h][u][t] = __builtin_amdgcn_ds_read_tr16_b64_v4i16((LAS s16x4*)(sb + va[u] + (2 * h + t) * 4096));
#pragma unroll
        for (int u = 0; u < 8; ++u) acc[u] = __builtin_amdgcn_mfma_f32_16x16x32_bf16(__builtin_bit_cast(bf16x8, __builtin_shufflevector(vf[0][u][0], vf[0][u][1], 0, 1, 2, 3, 4, 5, 6, 7)), pf0, acc[u], 0, 0, 0);
        asm volatile("s_waitcnt lgkmcnt(0)" : "+v"(vf[1][0][0]), "+v"(vf[1][0][1]), "+v"(vf[1][1][0]), "+v"(vf[1][1][1]), "+v"(vf[1][2][0]), "+v"(vf[1][2][1]), "+v"(vf[1][3][0]), "+v"(vf[1][3][1]),
                     "+v"(vf[1][4][0]), "+v"(vf[1][4][1]), "+v"(vf[1][5][0]), "+v"(vf[1][5][1]), "+v"(vf[1][6][0]), "+v"(vf[1][6][1]), "+v"(vf[1][7][0]), "+v"(vf[1][7][1]) :: "memory");
        CTL[C_FREE + s] = q + 1;
#pragma unroll
        for (int u = 0; u < 8; ++u) acc[u] = __builtin_amdgcn_mfma_f32_16x16x32_bf16(__builtin_bit_cast(bf16x8, __builtin_shufflevector(vf[1][u][0], vf[1][u][1], 0, 1, 2, 3, 4, 5, 6, 7)), pf1, acc[u], 0, 0, 0);
      }
    }
    lds_arrive(&CTL[C_CNTV], lane);
    {
      bf16x8 qf[4];
#pragma unroll
      for (int i = 0; i < 4; ++i) qf[i] = *(LAS bf16x8*)(L8 + L_QS + (n & 1) * 256 + i * 64 + G * 16);
      float ld = 0.f;
#pragma unroll 1
      for (int q = Q0 + ci;; q += NCONS) {
        const int s = q & (NSLOT - 1);
        bool go; u32 en;
        for (u32 sp = 0;;) {
          en = __builtin_amdgcn_readfirstlane(CTL[C_ISS]);
          go = (int)CTL[C_PUB] >= 2 * q + 1;
          if (go || (en >= 0x40000000u && q >= (int)(en - 0x40000000u)) || ++sp >= SPIN_LDS) break;
          __builtin_amdgcn_s_sleep(1);
        }
        if (!go) break;
        asm volatile("" ::: "memory");
        LAS unsigned char* sb = L8 + s * SLOTB;
        f32x4 sc[2];
#pragma unroll
        for (int t = 0; t < 2; ++t) {
          sc[t] = f32x4{0.f, 0.f, 0.f, 0.f};
#pragma unroll
          for (int i = 0; i < 4; ++i) sc[t] = __builtin_amdgcn_mfma_f32_16x16x32_bf16(*(LAS bf16x8*)(sb + ka[i] + t * 4096), qf[i], sc[t], 0, 0, 0);
        }
        float z[8];
#pragma unroll
        for (int t = 0; t < 2; ++t)
#pragma unroll
          for (int k = 0; k < 2; ++k) {
            const u32 a = pk2(sc[t][2 * k], sc[t][2 * k + 1]);
            const u32 c = pk2(bflo(a) * 0.08838834764831845f, bfhi(a) * 0.08838834764831845f);
            z[4 * t + 2 * k] = bflo(c) * 1.4426950408889634f; z[4 * t + 2 * k + 1] = bfhi(c) * 1.4426950408889634f;
          }
        const float zm = fmaxf(fmaxf(fmaxf(z[0], z[1]), fmaxf(z[2], z[3])), fmaxf(fmaxf(z[4], z[5]), fmaxf(z[6], z[7])));
        if (__any(zm > m)) {
          const float mn = fmaxf(m, ceilf(x32_max(x16_max(zm))));
          const float al = __builtin_amdgcn_exp2f(m - mn);
          l *= al; ld *= al; m = mn;
#pragma unroll
          for (int u = 0; u < 8; ++u) acc[u] *= al;
        }
        u32x4 pw;
#pragma unroll
        for (int k = 0; k < 4; ++k) { const float p0 = __builtin_amdgcn_exp2f(z[2 * k] - m), p1 = __builtin_amdgcn_exp2f(z[2 * k + 1] - m); ld += p0 + p1; pw[k] = pk2(p0, p1); }
        asm volatile("" : "+v"(pw));
        en = __builtin_amdgcn_readfirstlane(CTL[C_ISS]);
        { u32 sp = 0; while ((int)CTL[C_PUB] < 2 * q + 2 && ++sp < SPIN_LDS) {} asm volatile("" ::: "memory"); }
        s16x4 vf[8][2];
#pragma unroll
        for (int u = 0; u < 8; ++u)
#pragma unroll
          for (int t = 0; t < 2; ++t) vf[u][t] = __builtin_amdgcn_ds_read_tr16_b64_v4i16((LAS s16x4*)(sb + va[u] + (2 + t) * 4096));
#pragma unroll
        for (int u = 0; u < 8; ++u) acc[u] = __builtin_amdgcn_mfma_f32_16x16x32_bf16(__builtin_bit_cast(bf16x8, __builtin_shufflevector(vf[u][0], vf[u][1], 0, 1, 2, 3, 4, 5, 6, 7)), __builtin_bit_cast(bf16x8, pw), acc[u], 0, 0, 0);
        lds_wait();
        CTL[C_FREE + s] = q + 1;
        if (en >= 0x40000000u && q + NCONS >= (int)(en - 0x40000000u)) break;
      }
      l += x32_sum(x16_sum(ld));
    }
    {
      const bool u0 = n & 2, u1 = n & 4, u2 = n & 8;
      f32x4 o;
#pragma unroll
      for (int k = 0; k < 4; ++k) {
        const float b0 = sel(u0, acc[1][k], acc[0][k]), b1 = sel(u0, acc[3][k], acc[2][k]), b2 = sel(u0, acc[5][k], acc[4][k]), b3 = sel(u0, acc[7][k], acc[6][k]);
        o[k] = sel(u2, sel(u1, b3, b2), sel(u1, b1, b0));
      }
      LAS unsigned char* MR = L8 + L_MRG;
      *(volatile LAS f32x4*)(MR + ci * 1024 + lane * 16) = o;
      if (lane < 2) *(volatile LAS f32x2*)(MR + 4096 + ci * 16 + lane * 8) = f32x2{m, l};
      u32 old = 0;
      if (lane == 0) asm volatile("ds_add_rtn_u32 %0, %1, %2" : "=v"(old) : "v"((u32)(u64)&CTL[C_CNTM]), "v"(1u) : "memory");
      f32x4 oc[4]; f32x2 mc[4];
#pragma unroll
      for (int c = 0; c < 4; ++c) { oc[c] = *(volatile LAS f32x4*)(MR + c * 1024 + lane * 16); mc[c] = *(volatile LAS f32x2*)(MR + 4096 + c * 16 + (lane & 1) * 8); }
      asm volatile("s_waitcnt lgkmcnt(0)" : "+v"(old) :: "memory");
      if (__builtin_amdgcn_readfirstlane(old) == NCONS - 1) {
        const float M = fmaxf(fmaxf(mc[0][0], mc[1][0]), fmaxf(mc[2][0], mc[3][0]));
        f32x4 t = {0.f, 0.f, 0.f, 0.f}; float L = 0.f;
#pragma unroll
        for (int c = 0; c < 4; ++c) { const float w = __builtin_amdgcn_exp2f(mc[c][0] - M); t += w * oc[c]; L = fmaf(w, mc[c][1], L); }
        const u32 base = WS_P + (u32)((2 * g + (n & 1)) * 16 + wi) * P_STRIDE + ((n >> 1) * 4 + G) * 16;
        g_st(rws, base, asu(t[0]), asu(t[1]), tag);
        g_st(rws, base + 512, asu(t[2]), asu(t[3]), tag);
        if (lane < 2) g_st(rws, WS_ML + (u32)((2 * g + lane) * 16 + wi) * 16, asu(M), asu(L), tag);
        STAMPV(10, RT());
      }
    }
    if (ci == 0) STAMPV(8, tsr);
  }
}
}

extern "C" void kernel_launch(void* const* d_in, const int* in_sizes, int n_in, void* d_out, int out_size, void* d_ws, size_t ws_size, hipStream_t stream) {
  fk::Params p;
  p.x = (const u16*)d_in[0]; p.k = (const u16*)d_in[1]; p.v = (const u16*)d_in[2]; p.wq = (const u16*)d_in[3]; p.wo = (const u16*)d_in[4];
  p.wg = (const u16*)d_in[6]; p.wu = (const u16*)d_in[7]; p.wd = (const u16*)d_in[8];
  p.out = (u16*)d_out; p.ws = (unsigned char*)d_ws;
  hipLaunchKernelGGL(fk::fused_decode, dim3(fk::NWG), dim3(fk::NTHR), 0, stream, p);
}
```

```cpp
#include <hip/hip_runtime.h>
#include <hip/hip_bf16.h>
#include <stdint.h>

#define LAS __attribute__((address_space(3)))
#define GAS __attribute__((address_space(1)))
typedef unsigned u32;
typedef unsigned long long u64;
typedef unsigned short u16;
typedef unsigned u32x4 __attribute__((ext_vector_type(4)));
typedef unsigned u32x2 __attribute__((ext_vector_type(2)));
typedef float f32x4 __attribute__((ext_vector_type(4)));
typedef float f32x2 __attribute__((ext_vector_type(2)));
typedef __bf16 bf16x2 __attribute__((ext_vector_type(2)));
typedef __bf16 bf16x8 __attribute__((ext_vector_type(8)));
typedef short s16x4 __attribute__((ext_vector_type(4)));

namespace fk {
constexpr int DM = 576, SK = 65536, DH = 128, FF = 4608, NQ = 4096;
constexpr int NWG = 256, NTHR = 512;
constexpr int WN = 21;
constexpr int NSLOT = 8, SLOTB = 16384, NCONS = 4;
constexpr int NS = 60;
constexpr int Q0 = 2 * NS;
constexpr int NDT = (64 - NS) * 32;
constexpr int NSCH = 3, LEAD = 5;
constexpr int CODE_PF = 24000;
static_assert(WN > 16 && WN <= 24 && NS % 4 == 0 && NS >= 8 && NS < 64, "knobs");
static_assert(LEAD >= 5 && LEAD <= 10, "LEAD >= 5: the stream ends only after all three helpers have answered a ticket request");
constexpr u32 WS_E = 0, WS_CNT = 4096;
constexpr u32 WS_Q = 8192;
constexpr u32 WS_G = WS_Q + 4 * NQ * 8, WS_U = WS_G + 4 * FF * 8;
constexpr u32 WS_P = WS_U + 4 * FF * 8, P_STRIDE = 128 * 8;
constexpr u32 WS_ML = WS_P + 32 * 16 * P_STRIDE;
constexpr u32 WS_Y = WS_ML + 32 * 16 * 16;
constexpr u32 WS_F = WS_Y + 64 * DM * 8;
constexpr u32 WS_END = WS_F + 72 * DM * 8;
constexpr u32 L_CTL = NSLOT * SLOTB;
constexpr u32 L_QS = L_CTL + 256;
constexpr u32 L_OS = L_QS + 512;
constexpr u32 L_RS = L_OS + 128;
constexpr u32 L_MRG = L_RS;
constexpr u32 L_SC = L_MRG + 4224;
constexpr u32 L_END = L_SC + 64 * 384;
static_assert(L_END <= 163840 && L_END > 81920 && L_SC % 16 == 0 && NS * 256 <= 64 * 384, "LDS");
enum { C_PUB = 0, C_QRDY = 2, C_TAG = 3, C_CNTQ = 4, C_CNTF = 5, C_TK = 8, C_FREE = 16, C_ISS = 32, C_CNTV = 33, C_ORDY = 34, C_FOUT = 35, C_EMPTY = 36, C_CNTM = 37 };
constexpr u32 ENDM = 0x40000000u;
constexpr u32 SPIN_LDS = 1u << 22, SPIN_GLB = 1u << 19;
constexpr float SCALE = 0.08838834764831845f, LOG2E = 1.4426950408889634f;

struct Params {
  const u16 *k, *v, *x, *wq, *wo, *wg, *wu, *wd;
  u16* out;
  unsigned char* ws;
};

__device__ __forceinline__ float asf(u32 u) { return __uint_as_float(u); }
__device__ __forceinline__ u32 asu(float f) { return __float_as_uint(f); }
__device__ __forceinline__ float bflo(u32 u) { return asf(u << 16); }
__device__ __forceinline__ float bfhi(u32 u) { return asf(u & 0xffff0000u); }
__device__ __forceinline__ u32 pk2(float lo, float hi) { f32x2 f = {lo, hi}; return __builtin_bit_cast(u32, __builtin_convertvector(f, bf16x2)); }
__device__ __forceinline__ float rb(float f) { return bflo(pk2(f, 0.f) & 0xffffu); }
__device__ __forceinline__ float lg2u(float s) { float z = s * LOG2E; asm("" : "+v"(z)); return z; }
template <int CTRL> __device__ __forceinline__ float dpp(float x) {
  return __builtin_bit_cast(float, __builtin_amdgcn_mov_dpp(__builtin_bit_cast(int, x), CTRL, 0xf, 0xf, true));
}
constexpr int XOR1 = 0xB1, XOR2 = 0x4E, ROR4 = 0x124, ROR8 = 0x128;
__device__ __forceinline__ float x16_sum(float x) { auto s = __builtin_amdgcn_permlane16_swap(asu(x), asu(x), false, false); return asf(s[0]) + asf(s[1]); }
__device__ __forceinline__ float x32_sum(float x) { auto s = __builtin_amdgcn_permlane32_swap(asu(x), asu(x), false, false); return asf(s[0]) + asf(s[1]); }
__device__ __forceinline__ float x16_max(float x) { auto s = __builtin_amdgcn_permlane16_swap(asu(x), asu(x), false, false); return fmaxf(asf(s[0]), asf(s[1])); }
__device__ __forceinline__ float x32_max(float x) { auto s = __builtin_amdgcn_permlane32_swap(asu(x), asu(x), false, false); return fmaxf(asf(s[0]), asf(s[1])); }
__device__ __forceinline__ float row16_sum(float x) { x += dpp<XOR1>(x); x += dpp<XOR2>(x); x += dpp<ROR4>(x); x += dpp<ROR8>(x); return x; }
__device__ __forceinline__ float row16_max(float x) { x = fmaxf(x, dpp<XOR1>(x)); x = fmaxf(x, dpp<XOR2>(x)); x = fmaxf(x, dpp<ROR4>(x)); x = fmaxf(x, dpp<ROR8>(x)); return x; }
__device__ __forceinline__ float wave_sum(float x) { return x32_sum(x16_sum(row16_sum(x))); }
__device__ __forceinline__ float oct_sum(float x) { x += dpp<ROR8>(x); return x32_sum(x16_sum(x)); }
__device__ __forceinline__ float quad_sum(float x) { x += dpp<ROR4>(x); x += dpp<ROR8>(x); return x32_sum(x16_sum(x)); }
__device__ __forceinline__ float sel(bool c, float a, float b) { return c ? a : b; }
__device__ __forceinline__ bf16x8 cat(s16x4 lo, s16x4 hi) { return __builtin_bit_cast(bf16x8, __builtin_shufflevector(lo, hi, 0, 1, 2, 3, 4, 5, 6, 7)); }
__device__ __forceinline__ s16x4 tr_rd(LAS unsigned char* a) { return __builtin_amdgcn_ds_read_tr16_b64_v4i16((LAS s16x4*)a); }
__device__ __forceinline__ f32x4 mfma(bf16x8 a, bf16x8 b, f32x4 c) { return __builtin_amdgcn_mfma_f32_16x16x32_bf16(a, b, c, 0, 0, 0); }
__device__ __forceinline__ void lds_wait() { asm volatile("s_waitcnt lgkmcnt(0)" ::: "memory"); }
__device__ __forceinline__ void vm_drain() { asm volatile("s_waitcnt vmcnt(0)" ::: "memory"); }
__device__ __forceinline__ void wg_barrier() { asm volatile("s_waitcnt lgkmcnt(0)\n\ts_barrier" ::: "memory"); }
__device__ __forceinline__ u32x4 ldnt(const u16* p) { return __builtin_nontemporal_load((const GAS u32x4*)p); }
__device__ __forceinline__ u32x4 mk_srd(const void* p, u32 bytes) {
  const u64 a = (u64)p;
  u32x4 r; r.x = __builtin_amdgcn_readfirstlane((u32)a); r.y = __builtin_amdgcn_readfirstlane((u32)(a >> 32) & 0xffffu); r.z = bytes; r.w = 0x00020000u; return r;
}
__device__ __forceinline__ u32x4 g_ld(__amdgpu_buffer_rsrc_t r, u32 off) { asm volatile("" ::: "memory"); return __builtin_amdgcn_raw_buffer_load_b128(r, off, 0, 16); }
__device__ __forceinline__ void g_st(__amdgpu_buffer_rsrc_t r, u32 off, u32 a, u32 b, u32 tag) { u32x4 v = {a, tag, b, tag}; __builtin_amdgcn_raw_buffer_store_b128(v, r, off, 0, 16); }
constexpr u32 OOB = 0x80000000u;
template <int N, int SLEEP>
__device__ __forceinline__ void gather(__amdgpu_buffer_rsrc_t r, u32 (&off)[N], u32x4 (&v)[N], u32 tag) {
  for (u32 sp = 0;; ++sp) {
    u32x4 a[N]; u32 all = OOB;
#pragma unroll
    for (int i = 0; i < N; ++i) a[i] = g_ld(r, off[i]);
#pragma unroll
    for (int i = 0; i < N; ++i) {
      const bool ok = a[i][1] == tag && a[i][3] == tag && off[i] != OOB;
      v[i][0] = ok ? a[i][0] : v[i][0]; v[i][2] = ok ? a[i][2] : v[i][2]; off[i] = ok ? OOB : off[i]; all &= off[i];
    }
    if (__all(all == OOB) || sp >= SPIN_GLB) break;
    __builtin_amdgcn_s_sleep(SLEEP);
  }
}
template <int SLEEP>
__device__ __forceinline__ void lds_spin(volatile LAS u32* w, int need) {
  for (u32 sp = 0; (int)*w < need && sp < SPIN_LDS; ++sp) { if (SLEEP) __builtin_amdgcn_s_sleep(SLEEP); }
  asm volatile("" ::: "memory");
}
__device__ __forceinline__ u32 lds_arrive(volatile LAS u32* w, int lane) {
  lds_wait();
  u32 old = 0; if (lane == 0) old = __hip_atomic_fetch_add((LAS u32*)w, 1u, __ATOMIC_RELAXED, __HIP_MEMORY_SCOPE_WORKGROUP);
  old = __builtin_amdgcn_readfirstlane(old);
  asm volatile("" ::: "memory");
  return old;
}
template <int J0>
__device__ __forceinline__ void dma2x2(u32 la, u32 lb, u32 v0, u32 v1, u32x4 srd, u32 sa, u32 sb) {
  asm volatile("s_nop 4\n\ts_mov_b32 m0, %0\n\t"
               "s_waitcnt vmcnt(%c7)\n\t"
               "buffer_load_dwordx4 %2, %4, %5 offen offset:%c8 nt lds\n\t"
               "s_mov_b32 m0, %1\n\t"
               "s_waitcnt vmcnt(%c7)\n\t"
               "buffer_load_dwordx4 %2, %4, %6 offen offset:%c8 nt lds\n\t"
               "s_mov_b32 m0, %0\n\t"
               "s_waitcnt vmcnt(%c7)\n\t"
               "buffer_load_dwordx4 %3, %4, %5 offen offset:%c9 nt lds\n\t"
               "s_mov_b32 m0, %1\n\t"
               "s_waitcnt vmcnt(%c7)\n\t"
               "buffer_load_dwordx4 %3, %4, %6 offen offset:%c9 nt lds"
               :: "s"(la), "s"(lb), "v"(v0), "v"(v1), "s"(srd), "s"(sa), "s"(sb), "i"(WN - 1), "i"(J0 * 1024), "i"(J0 * 1024 + 1024) : "memory");
}
__device__ __forceinline__ void dma1(u32 ldsbase, u32 v0, u32x4 srd, u32 soff) {
  asm volatile("s_nop 4\n\ts_mov_b32 m0, %0\n\ts_nop 0\n\t"
               "buffer_load_dwordx4 %1, %2, %3 offen nt lds"
               :: "s"(ldsbase), "v"(v0), "s"(srd), "s"(soff) : "memory");
}
__device__ __forceinline__ void fma8(float (&acc)[8], u32x4 w, float xv) {
#pragma unroll
  for (int k = 0; k < 4; ++k) { acc[2 * k] = fmaf(xv, bflo(w[k]), acc[2 * k]); acc[2 * k + 1] = fmaf(xv, bfhi(w[k]), acc[2 * k + 1]); }
}
__device__ __forceinline__ u32 score2(float a, float b) { const u32 t = pk2(a, b); return pk2(bflo(t) * SCALE, bfhi(t) * SCALE); }

__global__ void __launch_bounds__(NTHR, 2) fused_decode(Params p) {
  __shared__ __attribute__((aligned(16))) unsigned char lds[L_END];
  LAS unsigned char* L8 = (LAS unsigned char*)lds;
  volatile LAS u32* CTL = (volatile LAS u32*)(L8 + L_CTL);
  const int b = blockIdx.x, tid = threadIdx.x, lane = tid & 63;
  const int wave = __builtin_amdgcn_readfirstlane(tid >> 6);
  u32 cpf0 = 0, cpf1 = 0;
  if (wave >= 4) {
    u64 pc; asm volatile("s_getpc_b64 %0" : "=s"(pc));
    const int o = (tid - 256) * 64;
    cpf0 = *(const GAS u32*)((pc & ~63ull) + o);
    cpf1 = *(const GAS u32*)((pc & ~63ull) + (o + 16384 < CODE_PF ? o + 16384 : o));
    __builtin_amdgcn_sched_barrier(0);
  }
  const int g = b >> 4, wi = b & 15;
  const int io = wi >= 4 ? (wi - 4) * 16 + g : 999, id = wi >= 2 ? (15 - wi) * 16 + g : 999, fi = (15 - wi) * 16 + g;
  const __amdgpu_buffer_rsrc_t rws = __builtin_amdgcn_make_buffer_rsrc(p.ws, 0, WS_END, 0x00020000);

  if (__builtin_expect(wave == 0, 1)) {
    __builtin_amdgcn_s_setprio(3);
    __builtin_amdgcn_s_waitcnt(0x0F70);
    const u32 r4 = lane >> 4, pc = lane & 15;
    const u32 ldsb = (u32)(u64)L8;
    int pub = 0, q = 0;
    auto wait_free = [&](int s, int need) {
      vm_drain(); pub = 2 * q; CTL[C_PUB] = pub;
      lds_spin<1>(&CTL[C_FREE + s], need);
    };
    auto publish = [&](int gr) {
      if (((gr * 4 + 4 - WN) % 8 + 8) % 8 < 4) { pub = max(pub, (q * 16 + gr * 4 + 4 - WN) >> 3); CTL[C_PUB] = pub; }
    };
    const u32x4 rk = mk_srd(p.k + (size_t)g * SK * DH, SK * DH * 2), rv = mk_srd(p.v + (size_t)g * SK * DH, SK * DH * 2);
    const u32 vk0 = r4 * 256 + ((pc ^ r4) << 4), vk1 = r4 * 256 + ((pc ^ (4 + r4)) << 4), vk2 = vk0 ^ 128u, vk3 = vk1 ^ 128u;
    const u32 vv0 = r4 * 256 + ((pc ^ (r4 << 1)) << 4), vv1 = r4 * 256 + ((pc ^ ((4 + r4) << 1)) << 4);
    const u32 fa = (u32)g << 19, fb = fa ^ (1u << 17);
#pragma unroll 1
    for (int ph = 0; ph < 2; ++ph) {
      const u32x4 rs = ph ? rv : rk;
      const u32 a0 = ph ? vv0 : vk0, a1 = ph ? vv1 : vk1, a2 = ph ? vv0 : vk2, a3 = ph ? vv1 : vk3;
#pragma unroll 1
      for (int i = 0; i < NS; ++i) {
        const u32 r = (u32)(wi + i) & 15u;
        const u32 so = ((u32)i << 17) | ((r >> 1) << 14) | ((r & 1u) << 12);
        const u32 so1 = so ^ fa, so2 = so1 ^ 0x3000u, so3 = (so ^ fb) + SK * DH, so4 = so3 ^ 0x3000u;
        const u32 lb = ldsb + (q & (NSLOT - 1)) * SLOTB;
        dma2x2<0>(lb, lb + 4096, a0, a1, rs, so1, so2);
        publish(0);
        if (q >= Q0 - 8) CTL[C_ISS] = 2 * q;
        dma2x2<2>(lb, lb + 4096, a2, a3, rs, so1, so2);
        publish(1);
        if (q == 1) wg_barrier();
        const u32 fr = CTL[C_FREE + ((q + 1) & (NSLOT - 1))];
        dma2x2<0>(lb + 8192, lb + 12288, a0, a1, rs, so3, so4);
        publish(2);
        if (q >= Q0 - 8) CTL[C_ISS] = 2 * q + 1;
        const bool full = q >= NSLOT - 1 && (int)__builtin_amdgcn_readfirstlane(fr) < q + 2 - NSLOT;
        dma2x2<2>(lb + 8192, lb + 12288, a2, a3, rs, so3, so4);
        publish(3);
        ++q;
        if (__builtin_expect(full, 0)) wait_free(q & (NSLOT - 1), q + 1 - NSLOT);
      }
    }
    {
      int x = Q0, sc = 0, xend = 0x7fffffff, dead = 0;
      u32 e = __builtin_amdgcn_readfirstlane(CTL[C_TK + (x & 7)]);
#pragma unroll 1
      for (;;) {
        if (__builtin_expect((int)(e >> 16) != x + 1 || (e & 0xffffu) >= (u32)NDT, 0)) {
          for (;;) {
            if (!((dead >> sc) & 1)) {
              e = __builtin_amdgcn_readfirstlane(CTL[C_TK + (x & 7)]);
              if ((int)(e >> 16) != x + 1) {
                u32 sp = 0;
                do {
                  if (sp == 0) { asm volatile("s_waitcnt vmcnt(16)" ::: "memory"); pub = max(pub, 2 * q - 2); }
                  else if (sp == 1) { asm volatile("s_waitcnt vmcnt(8)" ::: "memory"); pub = max(pub, 2 * q - 1); }
                  else { vm_drain(); pub = 2 * q; }
                  CTL[C_PUB] = pub;
                  e = __builtin_amdgcn_readfirstlane(CTL[C_TK + (x & 7)]);
                } while ((int)(e >> 16) != x + 1 && ++sp < SPIN_LDS);
                if (sp >= SPIN_LDS) e = 0xffffu;
              }
              if ((e & 0xffffu) < (u32)NDT) break;
              dead |= 1 << sc;
              if (xend == 0x7fffffff) xend = (2 * x - 1 + LEAD) >> 1;
            }
            ++x; sc = sc == NSCH - 1 ? 0 : sc + 1;
            if (x > xend) break;
          }
        }
        if (x > xend) break;
        ++x; sc = sc == NSCH - 1 ? 0 : sc + 1;
        const u32 t = e & 0xffffu, tl = t >> 1;
        const u32 so1 = ((((u32)NS << 17) + (((tl >> 1) << 14) | ((tl & 1u) << 12))) ^ ((t & 1u) ? fb : fa)) + (t & 1u) * (SK * DH), so2 = so1 ^ 0x3000u;
        const u32 lb = ldsb + (q & (NSLOT - 1)) * SLOTB;
        const bool ann = xend == 0x7fffffff;
        dma2x2<0>(lb, lb + 4096, vk0, vk1, rk, so1, so2);
        publish(0);
        if (ann) CTL[C_ISS] = 2 * q;
        dma2x2<2>(lb, lb + 4096, vk2, vk3, rk, so1, so2);
        publish(1);
        const u32 fr = CTL[C_FREE + ((q + 1) & (NSLOT - 1))];
        dma2x2<0>(lb + 8192, lb + 12288, vv0, vv1, rv, so1, so2);
        publish(2);
        if (ann) CTL[C_ISS] = 2 * q + 1;
        const bool full = (int)__builtin_amdgcn_readfirstlane(fr) < q + 2 - NSLOT;
        const u32 en = CTL[C_TK + (x & 7)];
        dma2x2<2>(lb + 8192, lb + 12288, vv0, vv1, rv, so1, so2);
        publish(3);
        e = __builtin_amdgcn_readfirstlane(en);
        ++q;
        if (__builtin_expect(full, 0)) wait_free(q & (NSLOT - 1), q + 1 - NSLOT);
        if (x > xend) break;
      }
    }
    CTL[C_ISS] = ENDM + (u32)q;
    asm volatile("s_waitcnt vmcnt(16)" ::: "memory"); pub = max(pub, 2 * q - 2); CTL[C_PUB] = pub;
    asm volatile("s_waitcnt vmcnt(8)" ::: "memory"); pub = max(pub, 2 * q - 1); CTL[C_PUB] = pub;
    vm_drain(); CTL[C_PUB] = 2 * q;
    __builtin_amdgcn_s_setprio(0);
    if (fi < 72) {
      const u32 tag = CTL[C_TAG];
      const int r16 = lane >> 2, e = lane & 3;
      const u32 off = (u32)(r16 * DM + 8 * fi + 2 * e) * 8;
      const u32 xx = *(const GAS u32*)(p.x + 8 * fi + 2 * e);
      u32x4 fv[5], yv[4];
      u32 fo[5] = {WS_F + off, WS_F + off + 16 * DM * 8, WS_F + off + 32 * DM * 8, WS_F + off + 48 * DM * 8, r16 < 8 ? WS_F + off + 64 * DM * 8 : OOB};
      u32 yo[4] = {WS_Y + off, WS_Y + off + 16 * DM * 8, WS_Y + off + 32 * DM * 8, WS_Y + off + 48 * DM * 8};
#pragma unroll
      for (int i = 0; i < 5; ++i) fv[i] = u32x4{0u, 0u, 0u, 0u};
#pragma unroll
      for (int i = 0; i < 4; ++i) yv[i] = u32x4{0u, 0u, 0u, 0u};
      lds_spin<1>(&CTL[C_FOUT], 1);
      __builtin_amdgcn_s_sleep(16);
      gather<5, 2>(rws, fo, fv, tag);
      float gt[2], r[2];
#pragma unroll
      for (int k = 0; k < 2; ++k) {
        const float f = rb(quad_sum(((asf(fv[0][2 * k]) + asf(fv[1][2 * k])) + (asf(fv[2][2 * k]) + asf(fv[3][2 * k]))) + asf(fv[4][2 * k])));
        gt[k] = rb(f / (1.f + __expf(-f)));
      }
      lds_spin<1>(&CTL[C_ORDY], 1);
      gather<4, 1>(rws, yo, yv, tag);
#pragma unroll
      for (int k = 0; k < 2; ++k) {
        const float sv = rb(quad_sum((asf(yv[0][2 * k]) + asf(yv[1][2 * k])) + (asf(yv[2][2 * k]) + asf(yv[3][2 * k]))));
        r[k] = (k ? bfhi(xx) : bflo(xx)) + rb(sv * gt[k]);
      }
      if (lane < 4) *(GAS u32*)(p.out + 8 * fi + 2 * e) = pk2(r[0], r[1]);
      if (fi == 0 && lane == 0) __hip_atomic_store((GAS u32*)(p.ws + WS_E), tag, __ATOMIC_RELAXED, __HIP_MEMORY_SCOPE_AGENT);
    }
  } else if (wave <= 3) {
    const int hw = wave - 1, tau = tid - 64;
    const int ch = tau & 7, rr = tau >> 3;
    const int kq = b & 3, row0 = kq * 144 + rr;
    const int cq = b >> 2;
    const u32 E = __hip_atomic_load((GAS u32*)(p.ws + WS_E), __ATOMIC_RELAXED, __HIP_MEMORY_SCOPE_AGENT);
    if (hw == 0 && wi == 0 && lane == 0) __hip_atomic_store((GAS u32*)(p.ws + WS_CNT + g * 256), 0u, __ATOMIC_RELAXED, __HIP_MEMORY_SCOPE_AGENT);
    asm volatile("" ::: "memory");
    u16 xs[9], xm[6];
#pragma unroll
    for (int i = 0; i < 9; ++i) xs[i] = p.x[lane + 64 * i];
#pragma unroll
    for (int i = 0; i < 6; ++i) xm[i] = p.x[row0 + 24 * i];
    u32x4 wa_[6], wb_[6];
#pragma unroll
    for (int i = 0; i < 6; ++i) wa_[i] = ldnt(p.wq + (size_t)(row0 + 24 * i) * NQ + cq * 64 + ch * 8);
    if (hw == 0) CTL[lane] = 0;
    __builtin_amdgcn_sched_barrier(0);
    wg_barrier();
    __builtin_amdgcn_sched_barrier(0);
    const u32 tag = __builtin_amdgcn_readfirstlane(E) + 1u;
    if (hw == 0 && lane == 0) CTL[C_TAG] = tag;
    float sum = 0.f;
#pragma unroll
    for (int i = 0; i < 9; ++i) sum += bflo(xs[i]);
    const float mean = wave_sum(sum) * (1.f / DM);
    float var = 0.f;
#pragma unroll
    for (int i = 0; i < 9; ++i) { const float d = bflo(xs[i]) - mean; var = fmaf(d, d, var); }
    var = wave_sum(var) * (1.f / DM);
    const float mb = rb(mean), den = rb(sqrtf(rb(rb(var) + 1e-5f)));
    float xn[6];
#pragma unroll
    for (int i = 0; i < 6; ++i) xn[i] = rb(rb(bflo(xm[i]) - mb) / den);
    LAS float* RS = (LAS float*)(L8 + L_RS);
    {
      float a[8] = {0.f, 0.f, 0.f, 0.f, 0.f, 0.f, 0.f, 0.f};
#pragma unroll
      for (int i = 0; i < 6; ++i) fma8(a, wa_[i], xn[i]);
#pragma unroll
      for (int k = 0; k < 8; ++k) a[k] = oct_sum(a[k]);
      if (lane < 8) { *(LAS f32x4*)(RS + hw * 64 + lane * 8) = f32x4{a[0], a[1], a[2], a[3]}; *(LAS f32x4*)(RS + hw * 64 + lane * 8 + 4) = f32x4{a[4], a[5], a[6], a[7]}; }
      vm_drain();
      if (lds_arrive(&CTL[C_CNTQ], lane) == 2 && lane < 32) {
        const f32x2 t = *(LAS f32x2*)(RS + lane * 2) + *(LAS f32x2*)(RS + 64 + lane * 2) + *(LAS f32x2*)(RS + 128 + lane * 2);
        g_st(rws, WS_Q + (u32)(kq * NQ + cq * 64 + lane * 2) * 8, asu(t[0]), asu(t[1]), tag);
      }
    }
#pragma unroll 1
    for (int pass = 0; pass < ((wi >> 1) == (g & 1) ? 2 : 1); ++pass) {
      const int cs = pass == 0 ? cq : 64 + (g >> 1);
#pragma unroll
      for (int i = 0; i < 6; ++i) { wa_[i] = ldnt(p.wg + (size_t)(row0 + 24 * i) * FF + cs * 64 + ch * 8); wb_[i] = ldnt(p.wu + (size_t)(row0 + 24 * i) * FF + cs * 64 + ch * 8); }
      float a[8] = {0.f, 0.f, 0.f, 0.f, 0.f, 0.f, 0.f, 0.f}, c[8] = {0.f, 0.f, 0.f, 0.f, 0.f, 0.f, 0.f, 0.f};
#pragma unroll
      for (int i = 0; i < 6; ++i) { fma8(a, wa_[i], xn[i]); fma8(c, wb_[i], xn[i]); }
#pragma unroll
      for (int k = 0; k < 8; ++k) { a[k] = oct_sum(a[k]); c[k] = oct_sum(c[k]); }
      LAS float* RG = RS + 192 + pass * 384; LAS float* RU = RG + 192;
      if (lane < 8) {
        *(LAS f32x4*)(RG + hw * 64 + lane * 8) = f32x4{a[0], a[1], a[2], a[3]}; *(LAS f32x4*)(RG + hw * 64 + lane * 8 + 4) = f32x4{a[4], a[5], a[6], a[7]};
        *(LAS f32x4*)(RU + hw * 64 + lane * 8) = f32x4{c[0], c[1], c[2], c[3]}; *(LAS f32x4*)(RU + hw * 64 + lane * 8 + 4) = f32x4{c[4], c[5], c[6], c[7]};
      }
      if (lds_arrive(&CTL[C_CNTF + pass], lane) == 2) {
        const LAS float* R = (lane < 32 ? RG : RU) + (lane & 31) * 2;
        const f32x2 t = *(const LAS f32x2*)R + *(const LAS f32x2*)(R + 64) + *(const LAS f32x2*)(R + 128);
        g_st(rws, (lane < 32 ? WS_G : WS_U) + (u32)(kq * FF + cs * 64 + (lane & 31) * 2) * 8, asu(t[0]), asu(t[1]), tag);
      }
    }
    const bool ptask = hw == 0 ? io < 192 : (hw == 1 && id < 216);
    const int rg = hw == 1 ? id % 72 : (io & 63), cg = hw == 1 ? id / 72 : io >> 6;
    u32x4 af[2];
    if (hw == 1 && ptask) {
      lds_spin<16>(&CTL[C_PUB], 48);
      u32x4 gu[8]; u32 of[8];
#pragma unroll
      for (int k = 0; k < 8; ++k) { of[k] = (k < 4 ? WS_G : WS_U) + (u32)((k & 3) * FF + 64 * rg + 2 * (lane & 31)) * 8; gu[k] = u32x4{0u, 0u, 0u, 0u}; }
      gather<8, 32>(rws, of, gu, tag);
      float hh[2];
#pragma unroll
      for (int k = 0; k < 2; ++k) {
        const float z = rb((asf(gu[0][2 * k]) + asf(gu[1][2 * k])) + (asf(gu[2][2 * k]) + asf(gu[3][2 * k])));
        const float u = rb((asf(gu[4][2 * k]) + asf(gu[5][2 * k])) + (asf(gu[6][2 * k]) + asf(gu[7][2 * k])));
        const float ge = rb(0.5f * z * (1.f + tanhf(0.7978845608028654f * (z + 0.044715f * z * z * z))));
        hh[k] = ge * u;
      }
      const u32 hp = pk2(hh[0], hh[1]);
#pragma unroll
      for (int ks = 0; ks < 2; ++ks)
#pragma unroll
        for (int i = 0; i < 4; ++i) af[ks][i] = __builtin_amdgcn_ds_bpermute((8 * i + 4 * ks + (lane >> 4)) * 4, hp);
    }
    lds_spin<4>(&CTL[C_QRDY], 1);
    for (int x = Q0 + hw;; x += NSCH) {
      int is; u32 sp = 0;
      while ((is = (int)CTL[C_ISS]) < 2 * x - LEAD && ++sp < SPIN_LDS) { if (is == 0) __builtin_amdgcn_s_sleep(16); else __builtin_amdgcn_s_sleep(1); }
      if (is >= (int)ENDM || sp >= SPIN_LDS) break;
      u32 t = 0xffffu;
      if (CTL[C_EMPTY] == 0) {
        if (lane == 0) t = __hip_atomic_fetch_add((GAS u32*)(p.ws + WS_CNT + g * 256), 1u, __ATOMIC_RELAXED, __HIP_MEMORY_SCOPE_AGENT);
        t = __builtin_amdgcn_readfirstlane(t);
        if (t > 0xffffu) t = 0xffffu;
      }
      CTL[C_TK + (x & 7)] = ((u32)(x + 1) << 16) | t;
      if (t >= (u32)NDT) { CTL[C_EMPTY] = 1; break; }
    }
    if (hw == 2 && io < 192) {
      const u32 off = WS_P + (u32)((rg >> 1) * 16 + (lane >> 3)) * P_STRIDE + (rg & 1) * 512 + (lane & 7) * 16;
      u32x4 d[9]; u32 of[9];
#pragma unroll
      for (int i = 0; i < 8; ++i) { of[i] = off + (i >> 2) * 8 * P_STRIDE + (i & 3) * 128; d[i] = u32x4{0u, 0u, 0u, 0u}; }
      of[8] = WS_ML + (u32)((rg >> 1) * 16 + (lane & 15)) * 16; d[8] = u32x4{0u, 0u, 0u, 0u};
      lds_spin<1>(&CTL[C_CNTM], NCONS);
      __builtin_amdgcn_s_sleep(10);
      gather<9, 1>(rws, of, d, tag);
      const float mj = asf(d[8][0]);
      const float wj = __builtin_amdgcn_exp2f(mj - row16_max(mj));
      const float sc = wj * __builtin_amdgcn_rcpf(row16_sum(wj * asf(d[8][2])));
      const float s0 = asf(__builtin_amdgcn_ds_bpermute((lane >> 3) * 4, asu(sc))), s1 = asf(__builtin_amdgcn_ds_bpermute((lane >> 3) * 4 + 32, asu(sc)));
      u32x4 o4;
#pragma unroll
      for (int i = 0; i < 4; ++i) o4[i] = pk2(oct_sum(fmaf(s0, asf(d[i][0]), s1 * asf(d[4 + i][0]))), oct_sum(fmaf(s0, asf(d[i][2]), s1 * asf(d[4 + i][2]))));
      if (lane < 8) *(volatile LAS u32x4*)(L8 + L_OS + lane * 16) = o4;
      CTL[C_ORDY] = 1;
    }
    if (ptask) {
      u32 lo = L_SC;
      if (hw == 1) {
        lds_spin<1>(&CTL[C_ISS], (int)ENDM);
        const int qa = ((int)(__builtin_amdgcn_readfirstlane(CTL[C_ISS]) - ENDM) + 1) & ~1;
        lds_spin<1>(&CTL[C_FREE + (qa & 7)], qa - 7); lds_spin<1>(&CTL[C_FREE + (qa & 7) + 1], qa - 6);
        lo = (u32)(qa & 7) * SLOTB;
      } else lds_spin<1>(&CTL[C_CNTV], NCONS);
      const int rs = hw == 0 ? 4 : 2, rbase = hw == 0 ? 128 * (rg >> 1) + 2 * (rg & 1) : 64 * rg;
      const u32x4 rw = mk_srd((hw == 0 ? p.wo : p.wd) + (size_t)rbase * DM + 192 * cg, ((31 * rs + 1) * DM + 192) * 2);
      u32 vo[3];
#pragma unroll
      for (int e = 0; e < 3; ++e) { const u32 u = (u32)(e * 64 + lane), row = u / 24u; vo[e] = ((row >> 1) * rs + (row & 1u)) * (DM * 2) + (u - row * 24u) * 16u; }
#pragma unroll 1
      for (int r = 0; r < 8; ++r)
#pragma unroll
        for (int e = 0; e < 3; ++e) dma1((u32)(u64)L8 + lo + r * 3072 + e * 1024, vo[e], rw, (u32)(4 * r * rs) * (DM * 2));
      vm_drain();
      LAS unsigned char* wa = L8 + lo + (16 * ((lane >> 3) & 1) + 2 * (lane >> 4) + ((lane >> 2) & 1)) * 384 + (lane & 3) * 8;
      bf16x8 wf[12][2];
#pragma unroll
      for (int t = 0; t < 12; ++t)
#pragma unroll
        for (int ks = 0; ks < 2; ++ks) wf[t][ks] = cat(tr_rd(wa + (8 * ks) * 384 + t * 32), tr_rd(wa + (8 * ks + 32) * 384 + t * 32));
      if (hw == 0) {
        lds_spin<0>(&CTL[C_ORDY], 1);
        af[0] = *(LAS u32x4*)(L8 + L_OS + (lane >> 4) * 16); af[1] = *(LAS u32x4*)(L8 + L_OS + 64 + (lane >> 4) * 16);
      }
      float y[12];
#pragma unroll
      for (int t = 0; t < 12; ++t) y[t] = mfma(__builtin_bit_cast(bf16x8, af[1]), wf[t][1], mfma(__builtin_bit_cast(bf16x8, af[0]), wf[t][0], f32x4{0.f, 0.f, 0.f, 0.f}))[0];
      const int G = lane >> 4;
      const bool odd = lane & 1, g0 = G & 1, g1 = G & 2;
      const float ya = sel(g1, sel(g0, y[6], y[4]), sel(g0, y[2], y[0])), yb = sel(g1, sel(g0, y[7], y[5]), sel(g0, y[3], y[1]));
      const float yc = sel(g0, y[10], y[8]), yd = sel(g0, y[11], y[9]);
      const float yan = dpp<XOR1>(ya), ybn = dpp<XOR1>(yb), ycn = dpp<XOR1>(yc), ydn = dpp<XOR1>(yd);
      const u32 base = (hw == 0 ? WS_Y : WS_F) + (u32)(rg * DM + 192 * cg + 32 * G + (odd ? 16 : 0) + (lane & 14)) * 8;
      g_st(rws, base, asu(odd ? ybn : ya), asu(odd ? yb : yan), tag);
      if (lane < 32) g_st(rws, base + 128 * 8, asu(odd ? ydn : yc), asu(odd ? yd : ycn), tag);
      if (hw == 1) CTL[C_FOUT] = 1;
    }
  } else {
    const int ci = wave - 4;
    const int n = lane & 15, G = lane >> 4;
    wg_barrier();
    asm volatile("" :: "v"(cpf0), "v"(cpf1));
    u32 ka[4], va[8];
#pragma unroll
    for (int i = 0; i < 4; ++i) ka[i] = n * 256 + (((4 * i + G) ^ n) << 4);
    bf16x8 kf[4][4];
    auto load_k = [&](int q) {
      lds_spin<1>(&CTL[C_PUB], 2 * q + 2);
      LAS unsigned char* sb = L8 + (q & (NSLOT - 1)) * SLOTB;
#pragma unroll
      for (int t = 0; t < 4; ++t)
#pragma unroll
        for (int i = 0; i < 4; ++i) kf[t][i] = *(LAS bf16x8*)(sb + ka[i] + t * 4096);
      asm volatile("s_waitcnt lgkmcnt(0)" : "+v"(kf[0][0]), "+v"(kf[0][1]), "+v"(kf[0][2]), "+v"(kf[0][3]), "+v"(kf[1][0]), "+v"(kf[1][1]), "+v"(kf[1][2]), "+v"(kf[1][3]),
                   "+v"(kf[2][0]), "+v"(kf[2][1]), "+v"(kf[2][2]), "+v"(kf[2][3]), "+v"(kf[3][0]), "+v"(kf[3][1]), "+v"(kf[3][2]), "+v"(kf[3][3]) :: "memory");
      CTL[C_FREE + (q & (NSLOT - 1))] = q + 1;
    };
    load_k(ci);
    lds_spin<2>(&CTL[C_CNTQ], 3);
    const u32 tag = CTL[C_TAG];
    if (ci == 0) {
      __builtin_amdgcn_s_sleep(8);
      u32x4 qv[8]; u32 of[8];
#pragma unroll
      for (int k = 0; k < 8; ++k) { of[k] = WS_Q + (u32)((k >> 1) * NQ + g * 256 + (k & 1) * 128 + 2 * lane) * 8; qv[k] = u32x4{0u, 0u, 0u, 0u}; }
      gather<8, 2>(rws, of, qv, tag);
#pragma unroll
      for (int j = 0; j < 2; ++j) {
        const float q0 = (asf(qv[j][0]) + asf(qv[2 + j][0])) + (asf(qv[4 + j][0]) + asf(qv[6 + j][0]));
        const float q1 = (asf(qv[j][2]) + asf(qv[2 + j][2])) + (asf(qv[4 + j][2]) + asf(qv[6 + j][2]));
        *(LAS u32*)(L8 + L_QS + j * 256 + lane * 4) = pk2(q0, q1);
      }
      lds_wait();
      CTL[C_QRDY] = 1;
    } else lds_spin<1>(&CTL[C_QRDY], 1);
    asm volatile("" ::: "memory");
    LAS unsigned char* SC = L8 + L_SC + ci * (NS / NCONS) * 256;
    float m, l;
    {
      bf16x8 qf[4][4];
#pragma unroll
      for (int i = 0; i < 4; ++i) {
        const u32x4 t4 = *(LAS u32x4*)(L8 + L_QS + (n & 1) * 256 + i * 64 + G * 16);
#pragma unroll
        for (int t = 0; t < 4; ++t) qf[t][i] = __builtin_bit_cast(bf16x8, (n >> 1) == t ? t4 : u32x4{0u, 0u, 0u, 0u});
      }
      const u32 sw = ((n >> 2) & 1) * 128 + (n & 1) * 64 + G * 16 + ((n >> 1) & 1) * 8;
      float mx = -INFINITY;
#pragma unroll 1
      for (int it = 0; it < NS / NCONS; ++it) {
        if (it > 0) load_k(it * NCONS + ci);
        f32x4 sa = {0.f, 0.f, 0.f, 0.f}, sb = {0.f, 0.f, 0.f, 0.f};
#pragma unroll
        for (int i = 0; i < 4; ++i) { sa = mfma(kf[0][i], qf[0][i], sa); sb = mfma(kf[2][i], qf[2][i], sb); sa = mfma(kf[1][i], qf[1][i], sa); sb = mfma(kf[3][i], qf[3][i], sb); }
        const f32x4 sc = sa + sb;
        const u32 c0 = score2(sc[0], sc[1]), c1 = score2(sc[2], sc[3]);
        mx = fmaxf(fmaxf(mx, fmaxf(bflo(c0), bfhi(c0))), fmaxf(bflo(c1), bfhi(c1)));
        if (n < 8) *(LAS u32x2*)(SC + it * 256 + sw) = u32x2{c0, c1};
      }
      mx = n < 8 ? mx : -INFINITY;
      mx = fmaxf(mx, dpp<XOR2>(mx)); mx = fmaxf(mx, dpp<ROR4>(mx)); mx = fmaxf(mx, dpp<ROR8>(mx)); mx = x32_max(x16_max(mx));
      m = ceilf(lg2u(mx));
      const float mo = dpp<XOR1>(m), mh = (((lane >> 2) ^ lane) & 1) ? mo : m;
      lds_wait();
      float ls = 0.f;
#pragma unroll
      for (int i = 0; i < (NS / NCONS + 3) / 4; ++i) {
        if (lane + 64 * i < NS / NCONS * 16) {
          LAS u32x4* cp = (LAS u32x4*)(SC + (lane + 64 * i) * 16);
          u32x4 z = *cp;
#pragma unroll
          for (int k = 0; k < 4; ++k) {
            const float p0 = __builtin_amdgcn_exp2f(lg2u(bflo(z[k])) - mh), p1 = __builtin_amdgcn_exp2f(lg2u(bfhi(z[k])) - mh);
            ls += p0 + p1; z[k] = pk2(p0, p1);
          }
          *cp = z;
        }
      }
      ls += dpp<XOR1>(ls); ls += dpp<XOR2>(ls); ls += dpp<ROR8>(ls); ls = x32_sum(x16_sum(ls));
      const float lo_ = dpp<ROR4>(ls);
      l = (((lane >> 2) ^ lane) & 1) ? lo_ : ls;
      lds_wait();
    }
    f32x4 acc[8];
    {
      const u32 row = 4 * G + (n >> 2), pp = n & 3;
#pragma unroll
      for (int u = 0; u < 8; ++u) { va[u] = row * 256 + ((((2 * u + (pp >> 1)) ^ ((row & 7) << 1))) << 4) + (pp & 1) * 8; acc[u] = f32x4{0.f, 0.f, 0.f, 0.f}; }
#pragma unroll 1
      for (int it = 0; it < NS / NCONS; ++it) {
        const int q = NS + it * NCONS + ci, s = q & (NSLOT - 1);
        const bf16x8 pf0 = *(LAS bf16x8*)(SC + it * 256 + (n & 1) * 64 + G * 16), pf1 = *(LAS bf16x8*)(SC + it * 256 + 128 + (n & 1) * 64 + G * 16);
        lds_spin<1>(&CTL[C_PUB], 2 * q + 2);
        LAS unsigned char* sb = L8 + s * SLOTB;
        s16x4 vf[2][8][2];
#pragma unroll
        for (int h = 0; h < 2; ++h)
#pragma unroll
          for (int u = 0; u < 8; ++u)
#pragma unroll
            for (int t = 0; t < 2; ++t) vf[h][u][t] = tr_rd(sb + va[u] + (2 * h + t) * 4096);
#pragma unroll
        for (int u = 0; u < 8; ++u) acc[u] = mfma(cat(vf[0][u][0], vf[0][u][1]), pf0, acc[u]);
        asm volatile("s_waitcnt lgkmcnt(0)" : "+v"(vf[1][0][0]), "+v"(vf[1][0][1]), "+v"(vf[1][1][0]), "+v"(vf[1][1][1]), "+v"(vf[1][2][0]), "+v"(vf[1][2][1]), "+v"(vf[1][3][0]), "+v"(vf[1][3][1]),
                     "+v"(vf[1][4][0]), "+v"(vf[1][4][1]), "+v"(vf[1][5][0]), "+v"(vf[1][5][1]), "+v"(vf[1][6][0]), "+v"(vf[1][6][1]), "+v"(vf[1][7][0]), "+v"(vf[1][7][1]) :: "memory");
        CTL[C_FREE + s] = q + 1;
#pragma unroll
        for (int u = 0; u < 8; ++u) acc[u] = mfma(cat(vf[1][u][0], vf[1][u][1]), pf1, acc[u]);
      }
    }
    lds_arrive(&CTL[C_CNTV], lane);
    {
      bf16x8 qf[4];
#pragma unroll
      for (int i = 0; i < 4; ++i) qf[i] = *(LAS bf16x8*)(L8 + L_QS + (n & 1) * 256 + i * 64 + G * 16);
      float ld = 0.f;
#pragma unroll 1
      for (int q = Q0 + ci;; q += NCONS) {
        const int s = q & (NSLOT - 1);
        bool go; u32 en;
        for (u32 sp = 0;;) {
          en = __builtin_amdgcn_readfirstlane(CTL[C_ISS]);
          go = (int)CTL[C_PUB] >= 2 * q + 1;
          if (go || (en >= ENDM && q >= (int)(en - ENDM)) || ++sp >= SPIN_LDS) break;
          __builtin_amdgcn_s_sleep(1);
        }
        if (!go) break;
        asm volatile("" ::: "memory");
        LAS unsigned char* sb = L8 + s * SLOTB;
        float z[8];
#pragma unroll
        for (int t = 0; t < 2; ++t) {
          f32x4 sc = {0.f, 0.f, 0.f, 0.f};
#pragma unroll
          for (int i = 0; i < 4; ++i) sc = mfma(*(LAS bf16x8*)(sb + ka[i] + t * 4096), qf[i], sc);
#pragma unroll
          for (int k = 0; k < 2; ++k) { const u32 c = score2(sc[2 * k], sc[2 * k + 1]); z[4 * t + 2 * k] = lg2u(bflo(c)); z[4 * t + 2 * k + 1] = lg2u(bfhi(c)); }
        }
        const float zm = fmaxf(fmaxf(fmaxf(z[0], z[1]), fmaxf(z[2], z[3])), fmaxf(fmaxf(z[4], z[5]), fmaxf(z[6], z[7])));
        if (__any(zm > m)) {
          const float mn = fmaxf(m, ceilf(x32_max(x16_max(zm))));
          const float al = __builtin_amdgcn_exp2f(m - mn);
          l *= al; ld *= al; m = mn;
#pragma unroll
          for (int u = 0; u < 8; ++u) acc[u] *= al;
        }
        u32x4 pw;
#pragma unroll
        for (int k = 0; k < 4; ++k) { const float p0 = __builtin_amdgcn_exp2f(z[2 * k] - m), p1 = __builtin_amdgcn_exp2f(z[2 * k + 1] - m); ld += p0 + p1; pw[k] = pk2(p0, p1); }
        asm volatile("" : "+v"(pw));
        en = __builtin_amdgcn_readfirstlane(CTL[C_ISS]);
        lds_spin<0>(&CTL[C_PUB], 2 * q + 2);
#pragma unroll
        for (int u = 0; u < 8; ++u) acc[u] = mfma(cat(tr_rd(sb + va[u] + 8192), tr_rd(sb + va[u] + 12288)), __builtin_bit_cast(bf16x8, pw), acc[u]);
        lds_wait();
        CTL[C_FREE + s] = q + 1;
        if (en >= ENDM && q + NCONS >= (int)(en - ENDM)) break;
      }
      l += x32_sum(x16_sum(ld));
    }
    {
      const bool u0 = n & 2, u1 = n & 4, u2 = n & 8;
      f32x4 o;
#pragma unroll
      for (int k = 0; k < 4; ++k) {
        const float b0 = sel(u0, acc[1][k], acc[0][k]), b1 = sel(u0, acc[3][k], acc[2][k]), b2 = sel(u0, acc[5][k], acc[4][k]), b3 = sel(u0, acc[7][k], acc[6][k]);
        o[k] = sel(u2, sel(u1, b3, b2), sel(u1, b1, b0));
      }
      LAS unsigned char* MR = L8 + L_MRG;
      *(volatile LAS f32x4*)(MR + ci * 1024 + lane * 16) = o;
      if (lane < 2) *(volatile LAS f32x2*)(MR + 4096 + ci * 16 + lane * 8) = f32x2{m, l};
      u32 old = 0;
      if (lane == 0) asm volatile("ds_add_rtn_u32 %0, %1, %2" : "=v"(old) : "v"((u32)(u64)&CTL[C_CNTM]), "v"(1u) : "memory");
      f32x4 oc[4]; f32x2 mc[4];
#pragma unroll
      for (int c = 0; c < 4; ++c) { oc[c] = *(volatile LAS f32x4*)(MR + c * 1024 + lane * 16); mc[c] = *(volatile LAS f32x2*)(MR + 4096 + c * 16 + (lane & 1) * 8); }
      asm volatile("s_waitcnt lgkmcnt(0)" : "+v"(old) :: "memory");
      if (__builtin_amdgcn_readfirstlane(old) == NCONS - 1) {
        const float M = fmaxf(fmaxf(mc[0][0], mc[1][0]), fmaxf(mc[2][0], mc[3][0]));
        f32x4 t = {0.f, 0.f, 0.f, 0.f}; float L = 0.f;
#pragma unroll
        for (int c = 0; c < 4; ++c) { const float w = __builtin_amdgcn_exp2f(mc[c][0] - M); t += w * oc[c]; L = fmaf(w, mc[c][1], L); }
        const u32 base = WS_P + (u32)((2 * g + (n & 1)) * 16 + wi) * P_STRIDE + ((n >> 1) * 4 + G) * 16;
        g_st(rws, base, asu(t[0]), asu(t[1]), tag);
        g_st(rws, base + 512, asu(t[2]), asu(t[3]), tag);
        if (lane < 2) g_st(rws, WS_ML + (u32)((2 * g + lane) * 16 + wi) * 16, asu(M), asu(L), tag);
      }
    }
  }
}
}

extern "C" void kernel_launch(void* const* d_in, const int* in_sizes, int n_in, void* d_out, int out_size, void* d_ws, size_t ws_size, hipStream_t stream) {
  fk::Params p;
  p.x = (const u16*)d_in[0]; p.k = (const u16*)d_in[1]; p.v = (const u16*)d_in[2]; p.wq = (const u16*)d_in[3]; p.wo = (const u16*)d_in[4];
  p.wg = (const u16*)d_in[6]; p.wu = (const u16*)d_in[7]; p.wd = (const u16*)d_in[8];
  p.out = (u16*)d_out; p.ws = (unsigned char*)d_ws;
  hipLaunchKernelGGL(fk::fused_decode, dim3(fk::NWG), dim3(fk::NTHR), 0, stream, p);
}
```

```cpp
#include <hip/hip_runtime.h>
#include <hip/hip_bf16.h>
#include <stdint.h>

#define LAS __attribute__((address_space(3)))
#define GAS __attribute__((address_space(1)))
typedef unsigned u32;
typedef unsigned long long u64;
typedef unsigned short u16;
typedef unsigned u32x4 __attribute__((ext_vector_type(4)));
typedef unsigned u32x2 __attribute__((ext_vector_type(2)));
typedef float f32x4 __attribute__((ext_vector_type(4)));
typedef float f32x2 __attribute__((ext_vector_type(2)));
typedef __bf16 bf16x2 __attribute__((ext_vector_type(2)));
typedef __bf16 bf16x8 __attribute__((ext_vector_type(8)));
typedef short s16x4 __attribute__((ext_vector_type(4)));

namespace fk {
constexpr int DM = 576, SK = 65536, DH = 128, FF = 4608, NQ = 4096;
constexpr int NWG = 256, NTHR = 512;
constexpr int WN = 21;
constexpr int NSLOT = 8, SLOTB = 16384, NCONS = 4;
constexpr int NS = 60;
constexpr int Q0 = 2 * NS;
constexpr int NDT = (64 - NS) * 32;
constexpr int NSCH = 3, LEAD = 5;
constexpr int CODE_PF = 22528;
static_assert(WN > 16 && WN <= 24 && NS % 4 == 0 && NS >= 8 && NS < 64, "knobs");
static_assert(LEAD >= 5 && LEAD <= 10, "LEAD >= 5: the stream ends only after all three helpers have answered a ticket request");
constexpr u32 WS_E = 0, WS_CNT = 4096;
constexpr u32 WS_Q = 8192;
constexpr u32 WS_G = WS_Q + 4 * NQ * 8, WS_U = WS_G + 4 * FF * 8;
constexpr u32 WS_P = WS_U + 4 * FF * 8, P_STRIDE = 128 * 8;
constexpr u32 WS_ML = WS_P + 32 * 16 * P_STRIDE;
constexpr u32 WS_Y = WS_ML + 32 * 16 * 16;
constexpr u32 WS_F = WS_Y + 64 * DM * 8;
constexpr u32 WS_END = WS_F + 72 * DM * 8;
constexpr u32 L_CTL = NSLOT * SLOTB;
constexpr u32 L_QS = L_CTL + 256;
constexpr u32 L_OS = L_QS + 512;
constexpr u32 L_RS = L_OS + 128;
constexpr u32 L_MRG = L_RS;
constexpr u32 L_SC = L_MRG + 4224;
constexpr u32 L_END = L_SC + 64 * 384;
static_assert(L_END <= 163840 && L_END > 81920 && L_SC % 16 == 0 && NS * 256 <= 64 * 384, "LDS");
enum { C_PUB = 0, C_QRDY = 2, C_TAG = 3, C_CNTQ = 4, C_CNTF = 5, C_TK = 8, C_FREE = 16, C_ISS = 32, C_CNTV = 33, C_ORDY = 34, C_FOUT = 35, C_EMPTY = 36, C_CNTM = 37 };
constexpr u32 ENDM = 0x40000000u;
constexpr u32 SPIN_LDS = 1u << 22, SPIN_GLB = 1u << 18;
constexpr float SCALE = 0.08838834764831845f, LOG2E = 1.4426950408889634f;

struct Params {
  const u16 *k, *v, *x, *wq, *wo, *wg, *wu, *wd;
  u16* out;
  unsigned char* ws;
};

__device__ __forceinline__ float asf(u32 u) { return __uint_as_float(u); }
__device__ __forceinline__ u32 asu(float f) { return __float_as_uint(f); }
__device__ __forceinline__ float bflo(u32 u) { return asf(u << 16); }
__device__ __forceinline__ float bfhi(u32 u) { return asf(u & 0xffff0000u); }
__device__ __forceinline__ u32 pk2(float lo, float hi) { f32x2 f = {lo, hi}; return __builtin_bit_cast(u32, __builtin_convertvector(f, bf16x2)); }
__device__ __forceinline__ float rb(float f) { return bflo(pk2(f, 0.f) & 0xffffu); }
__device__ __forceinline__ float lg2u(float s) { float z = s * LOG2E; asm("" : "+v"(z)); return z; }
template <int CTRL> __device__ __forceinline__ float dpp(float x) {
  return __builtin_bit_cast(float, __builtin_amdgcn_mov_dpp(__builtin_bit_cast(int, x), CTRL, 0xf, 0xf, true));
}
constexpr int XOR1 = 0xB1, XOR2 = 0x4E, ROR4 = 0x124, ROR8 = 0x128;
__device__ __forceinline__ float x16_sum(float x) { auto s = __builtin_amdgcn_permlane16_swap(asu(x), asu(x), false, false); return asf(s[0]) + asf(s[1]); }
__device__ __forceinline__ float x32_sum(float x) { auto s = __builtin_amdgcn_permlane32_swap(asu(x), asu(x), false, false); return asf(s[0]) + asf(s[1]); }
__device__ __forceinline__ float x16_max(float x) { auto s = __builtin_amdgcn_permlane16_swap(asu(x), asu(x), false, false); return fmaxf(asf(s[0]), asf(s[1])); }
__device__ __forceinline__ float x32_max(float x) { auto s = __builtin_amdgcn_permlane32_swap(asu(x), asu(x), false, false); return fmaxf(asf(s[0]), asf(s[1])); }
__device__ __forceinline__ float row16_sum(float x) { x += dpp<XOR1>(x); x += dpp<XOR2>(x); x += dpp<ROR4>(x); x += dpp<ROR8>(x); return x; }
__device__ __forceinline__ float row16_max(float x) { x = fmaxf(x, dpp<XOR1>(x)); x = fmaxf(x, dpp<XOR2>(x)); x = fmaxf(x, dpp<ROR4>(x)); x = fmaxf(x, dpp<ROR8>(x)); return x; }
__device__ __forceinline__ float wave_sum(float x) { return x32_sum(x16_sum(row16_sum(x))); }
__device__ __forceinline__ float oct_sum(float x) { x += dpp<ROR8>(x); return x32_sum(x16_sum(x)); }
__device__ __forceinline__ float quad_sum(float x) { x += dpp<ROR4>(x); x += dpp<ROR8>(x); return x32_sum(x16_sum(x)); }
__device__ __forceinline__ float sel(bool c, float a, float b) { return c ? a : b; }
__device__ __forceinline__ bf16x8 cat(s16x4 lo, s16x4 hi) { return __builtin_bit_cast(bf16x8, __builtin_shufflevector(lo, hi, 0, 1, 2, 3, 4, 5, 6, 7)); }
__device__ __forceinline__ s16x4 tr_rd(LAS unsigned char* a) { return __builtin_amdgcn_ds_read_tr16_b64_v4i16((LAS s16x4*)a); }
__device__ __forceinline__ f32x4 mfma(bf16x8 a, bf16x8 b, f32x4 c) { return __builtin_amdgcn_mfma_f32_16x16x32_bf16(a, b, c, 0, 0, 0); }
__device__ __forceinline__ void lds_wait() { asm volatile("s_waitcnt lgkmcnt(0)" ::: "memory"); }
__device__ __forceinline__ void vm_drain() { asm volatile("s_waitcnt vmcnt(0)" ::: "memory"); }
__device__ __forceinline__ void wg_barrier() { asm volatile("s_waitcnt lgkmcnt(0)\n\ts_barrier" ::: "memory"); }
__device__ __forceinline__ u32x4 ldnt(const u16* p) { return __builtin_nontemporal_load((const GAS u32x4*)p); }
__device__ __forceinline__ u32x4 mk_srd(const void* p, u32 bytes) {
  const u64 a = (u64)p;
  u32x4 r; r.x = __builtin_amdgcn_readfirstlane((u32)a); r.y = __builtin_amdgcn_readfirstlane((u32)(a >> 32) & 0xffffu); r.z = bytes; r.w = 0x00020000u; return r;
}
__device__ __forceinline__ u32x4 g_ld(__amdgpu_buffer_rsrc_t r, u32 off) { asm volatile("" ::: "memory"); return __builtin_amdgcn_raw_buffer_load_b128(r, off, 0, 16); }
__device__ __forceinline__ void g_st(__amdgpu_buffer_rsrc_t r, u32 off, u32 a, u32 b, u32 tag) { u32x4 v = {a, tag, b, tag}; __builtin_amdgcn_raw_buffer_store_b128(v, r, off, 0, 16); }
constexpr u32 OOB = 0x80000000u;
template <int N, int SLEEP>
__device__ __forceinline__ void gather(__amdgpu_buffer_rsrc_t r, u32 (&off)[N], u32x4 (&v)[N], u32 tag) {
  for (u32 sp = 0;; ++sp) {
    u32x4 a[N]; u32 all = OOB;
#pragma unroll
    for (int i = 0; i < N; ++i) a[i] = g_ld(r, off[i]);
#pragma unroll
    for (int i = 0; i < N; ++i) {
      const bool ok = a[i][1] == tag && a[i][3] == tag && off[i] != OOB;
      v[i][0] = ok ? a[i][0] : v[i][0]; v[i][2] = ok ? a[i][2] : v[i][2]; off[i] = ok ? OOB : off[i]; all &= off[i];
    }
    if (__all(all == OOB) || sp >= SPIN_GLB) break;
    if (SLEEP) __builtin_amdgcn_s_sleep(SLEEP);
  }
}
template <int SLEEP>
__device__ __forceinline__ void lds_spin(volatile LAS u32* w, int need) {
  for (u32 sp = 0; (int)*w < need && sp < SPIN_LDS / (SLEEP + 1); ++sp) { if (SLEEP) __builtin_amdgcn_s_sleep(SLEEP); }
  asm volatile("" ::: "memory");
}
__device__ __forceinline__ u32 lds_arrive(volatile LAS u32* w, int lane) {
  lds_wait();
  u32 old = 0; if (lane == 0) old = __hip_atomic_fetch_add((LAS u32*)w, 1u, __ATOMIC_RELAXED, __HIP_MEMORY_SCOPE_WORKGROUP);
  old = __builtin_amdgcn_readfirstlane(old);
  asm volatile("" ::: "memory");
  return old;
}
template <int J0>
__device__ __forceinline__ void dma2x2(u32 la, u32 lb, u32 v0, u32 v1, u32x4 srd, u32 sa, u32 sb) {
  asm volatile("s_nop 4\n\ts_mov_b32 m0, %0\n\t"
               "s_waitcnt vmcnt(%c7)\n\t"
               "buffer_load_dwordx4 %2, %4, %5 offen offset:%c8 nt lds\n\t"
               "s_mov_b32 m0, %1\n\t"
               "s_waitcnt vmcnt(%c7)\n\t"
               "buffer_load_dwordx4 %2, %4, %6 offen offset:%c8 nt lds\n\t"
               "s_mov_b32 m0, %0\n\t"
               "s_waitcnt vmcnt(%c7)\n\t"
               "buffer_load_dwordx4 %3, %4, %5 offen offset:%c9 nt lds\n\t"
               "s_mov_b32 m0, %1\n\t"
               "s_waitcnt vmcnt(%c7)\n\t"
               "buffer_load_dwordx4 %3, %4, %6 offen offset:%c9 nt lds"
               :: "s"(la), "s"(lb), "v"(v0), "v"(v1), "s"(srd), "s"(sa), "s"(sb), "i"(WN - 1), "i"(J0 * 1024), "i"(J0 * 1024 + 1024) : "memory");
}
__device__ __forceinline__ void dma1(u32 ldsbase, u32 v0, u32x4 srd, u32 soff) {
  asm volatile("s_nop 4\n\ts_mov_b32 m0, %0\n\ts_nop 0\n\t"
               "buffer_load_dwordx4 %1, %2, %3 offen nt lds"
               :: "s"(ldsbase), "v"(v0), "s"(srd), "s"(soff) : "memory");
}
__device__ __forceinline__ void fma8(float (&acc)[8], u32x4 w, float xv) {
#pragma unroll
  for (int k = 0; k < 4; ++k) { acc[2 * k] = fmaf(xv, bflo(w[k]), acc[2 * k]); acc[2 * k + 1] = fmaf(xv, bfhi(w[k]), acc[2 * k + 1]); }
}
__device__ __forceinline__ u32 score2(float a, float b) { const u32 t = pk2(a, b); return pk2(bflo(t) * SCALE, bfhi(t) * SCALE); }

__global__ void __launch_bounds__(NTHR, 2) fused_decode(Params p) {
  __shared__ __attribute__((aligned(16))) unsigned char lds[L_END];
  LAS unsigned char* L8 = (LAS unsigned char*)lds;
  volatile LAS u32* CTL = (volatile LAS u32*)(L8 + L_CTL);
  const int b = blockIdx.x, tid = threadIdx.x, lane = tid & 63;
  const int wave = __builtin_amdgcn_readfirstlane(tid >> 6);
  u32 cpf0 = 0, cpf1 = 0;
  if (wave >= 4) {
    u64 pc; asm volatile("s_getpc_b64 %0" : "=s"(pc));
    const int o = (tid - 256) * 64;
    cpf0 = *(const GAS u32*)((pc & ~63ull) + o);
    cpf1 = *(const GAS u32*)((pc & ~63ull) + (o + 16384 < CODE_PF ? o + 16384 : o));
    __builtin_amdgcn_sched_barrier(0);
  }
  const int g = b >> 4, wi = b & 15;
  const int io = wi >= 4 ? (wi - 4) * 16 + g : 999, id = wi >= 2 ? (15 - wi) * 16 + g : 999, fi = (15 - wi) * 16 + g;
  const __amdgpu_buffer_rsrc_t rws = __builtin_amdgcn_make_buffer_rsrc(p.ws, 0, WS_END, 0x00020000);

  if (__builtin_expect(wave == 0, 1)) {
    __builtin_amdgcn_s_setprio(3);
    __builtin_amdgcn_s_waitcnt(0x0F70);
    const u32 r4 = lane >> 4, pc = lane & 15;
    const u32 ldsb = (u32)(u64)L8;
    int pub = 0, q = 0;
    auto wait_free = [&](int s, int need) {
      vm_drain(); pub = 2 * q; CTL[C_PUB] = pub;
      lds_spin<1>(&CTL[C_FREE + s], need);
    };
    auto publish = [&](int gr) {
      if (((gr * 4 + 4 - WN) % 8 + 8) % 8 < 4) { pub = max(pub, (q * 16 + gr * 4 + 4 - WN) >> 3); CTL[C_PUB] = pub; }
    };
    const u32x4 rk = mk_srd(p.k + (size_t)g * SK * DH, SK * DH * 2), rv = mk_srd(p.v + (size_t)g * SK * DH, SK * DH * 2);
    const u32 vk0 = r4 * 256 + ((pc ^ r4) << 4), vk1 = r4 * 256 + ((pc ^ (4 + r4)) << 4), vk2 = vk0 ^ 128u, vk3 = vk1 ^ 128u;
    const u32 vv0 = r4 * 256 + ((pc ^ (r4 << 1)) << 4), vv1 = r4 * 256 + ((pc ^ ((4 + r4) << 1)) << 4);
    const u32 fa = (u32)g << 19, fb = fa ^ (1u << 17);
#pragma unroll 1
    for (int ph = 0; ph < 2; ++ph) {
      const u32x4 rs = ph ? rv : rk;
      const u32 a0 = ph ? vv0 : vk0, a1 = ph ? vv1 : vk1, a2 = ph ? vv0 : vk2, a3 = ph ? vv1 : vk3;
#pragma unroll 1
      for (int i = 0; i < NS; ++i) {
        const u32 r = (u32)(wi + i) & 15u;
        const u32 so = ((u32)i << 17) | ((r >> 1) << 14) | ((r & 1u) << 12);
        const u32 so1 = so ^ fa, so2 = so1 ^ 0x3000u, so3 = (so ^ fb) + SK * DH, so4 = so3 ^ 0x3000u;
        const u32 lb = ldsb + (q & (NSLOT - 1)) * SLOTB;
        dma2x2<0>(lb, lb + 4096, a0, a1, rs, so1, so2);
        publish(0);
        if (q >= Q0 - 8) CTL[C_ISS] = 2 * q;
        dma2x2<2>(lb, lb + 4096, a2, a3, rs, so1, so2);
        publish(1);
        if (q == 1) wg_barrier();
        const u32 fr = CTL[C_FREE + ((q + 1) & (NSLOT - 1))];
        dma2x2<0>(lb + 8192, lb + 12288, a0, a1, rs, so3, so4);
        publish(2);
        if (q >= Q0 - 8) CTL[C_ISS] = 2 * q + 1;
        const bool full = q >= NSLOT - 1 && (int)__builtin_amdgcn_readfirstlane(fr) < q + 2 - NSLOT;
        dma2x2<2>(lb + 8192, lb + 12288, a2, a3, rs, so3, so4);
        publish(3);
        ++q;
        if (__builtin_expect(full, 0)) wait_free(q & (NSLOT - 1), q + 1 - NSLOT);
      }
    }
    {
      int x = Q0, sc = 0, xend = 0x7fffffff, dead = 0;
      u32 e = __builtin_amdgcn_readfirstlane(CTL[C_TK + (x & 7)]);
#pragma unroll 1
      for (;;) {
        if (__builtin_expect((int)(e >> 16) != x + 1 || (e & 0xffffu) >= (u32)NDT, 0)) {
          for (;;) {
            if (!((dead >> sc) & 1)) {
              e = __builtin_amdgcn_readfirstlane(CTL[C_TK + (x & 7)]);
              if ((int)(e >> 16) != x + 1) {
                u32 sp = 0;
                do {
                  if (sp == 0) { asm volatile("s_waitcnt vmcnt(16)" ::: "memory"); pub = max(pub, 2 * q - 2); }
                  else if (sp == 1) { asm volatile("s_waitcnt vmcnt(8)" ::: "memory"); pub = max(pub, 2 * q - 1); }
                  else { vm_drain(); pub = 2 * q; }
                  CTL[C_PUB] = pub;
                  e = __builtin_amdgcn_readfirstlane(CTL[C_TK + (x & 7)]);
                } while ((int)(e >> 16) != x + 1 && ++sp < SPIN_LDS);
                if (sp >= SPIN_LDS) e = 0xffffu;
              }
              if ((e & 0xffffu) < (u32)NDT) break;
              dead |= 1 << sc;
              if (xend == 0x7fffffff) xend = (2 * x - 1 + LEAD) >> 1;
            }
            ++x; sc = sc == NSCH - 1 ? 0 : sc + 1;
            if (x > xend) break;
          }
        }
        if (x > xend) break;
        ++x; sc = sc == NSCH - 1 ? 0 : sc + 1;
        const u32 t = e & 0xffffu, tl = t >> 1;
        const u32 so1 = ((((u32)NS << 17) + (((tl >> 1) << 14) | ((tl & 1u) << 12))) ^ ((t & 1u) ? fb : fa)) + (t & 1u) * (SK * DH), so2 = so1 ^ 0x3000u;
        const u32 lb = ldsb + (q & (NSLOT - 1)) * SLOTB;
        const bool ann = xend == 0x7fffffff;
        dma2x2<0>(lb, lb + 4096, vk0, vk1, rk, so1, so2);
        publish(0);
        if (ann) CTL[C_ISS] = 2 * q;
        dma2x2<2>(lb, lb + 4096, vk2, vk3, rk, so1, so2);
        publish(1);
        const u32 fr = CTL[C_FREE + ((q + 1) & (NSLOT - 1))];
        dma2x2<0>(lb + 8192, lb + 12288, vv0, vv1, rv, so1, so2);
        publish(2);
        if (ann) CTL[C_ISS] = 2 * q + 1;
        const bool full = (int)__builtin_amdgcn_readfirstlane(fr) < q + 2 - NSLOT;
        const u32 en = CTL[C_TK + (x & 7)];
        dma2x2<2>(lb + 8192, lb + 12288, vv0, vv1, rv, so1, so2);
        publish(3);
        e = __builtin_amdgcn_readfirstlane(en);
        ++q;
        if (__builtin_expect(full, 0)) wait_free(q & (NSLOT - 1), q + 1 - NSLOT);
        if (x > xend) break;
      }
    }
    CTL[C_ISS] = ENDM + (u32)q;
    asm volatile("s_waitcnt vmcnt(16)" ::: "memory"); pub = max(pub, 2 * q - 2); CTL[C_PUB] = pub;
    asm volatile("s_waitcnt vmcnt(8)" ::: "memory"); pub = max(pub, 2 * q - 1); CTL[C_PUB] = pub;
    vm_drain(); CTL[C_PUB] = 2 * q;
    __builtin_amdgcn_s_setprio(0);
    if (fi < 72) {
      const u32 tag = CTL[C_TAG];
      const int r16 = lane >> 2, e = lane & 3;
      const u32 off = (u32)(r16 * DM + 8 * fi + 2 * e) * 8;
      const u32 xx = *(const GAS u32*)(p.x + 8 * fi + 2 * e);
      u32x4 fv[5], yv[4];
      u32 fo[5] = {WS_F + off, WS_F + off + 16 * DM * 8, WS_F + off + 32 * DM * 8, WS_F + off + 48 * DM * 8, r16 < 8 ? WS_F + off + 64 * DM * 8 : OOB};
      u32 yo[4] = {WS_Y + off, WS_Y + off + 16 * DM * 8, WS_Y + off + 32 * DM * 8, WS_Y + off + 48 * DM * 8};
#pragma unroll
      for (int i = 0; i < 5; ++i) fv[i] = u32x4{0u, 0u, 0u, 0u};
#pragma unroll
      for (int i = 0; i < 4; ++i) yv[i] = u32x4{0u, 0u, 0u, 0u};
      lds_spin<1>(&CTL[C_FOUT], 1);
      __builtin_amdgcn_s_sleep(16);
      gather<5, 2>(rws, fo, fv, tag);
      float gt[2], r[2];
#pragma unroll
      for (int k = 0; k < 2; ++k) {
        const float f = rb(quad_sum(((asf(fv[0][2 * k]) + asf(fv[1][2 * k])) + (asf(fv[2][2 * k]) + asf(fv[3][2 * k]))) + asf(fv[4][2 * k])));
        gt[k] = rb(f / (1.f + __expf(-f)));
      }
      lds_spin<1>(&CTL[C_ORDY], 1);
      gather<4, 0>(rws, yo, yv, tag);
#pragma unroll
      for (int k = 0; k < 2; ++k) {
        const float sv = rb(quad_sum((asf(yv[0][2 * k]) + asf(yv[1][2 * k])) + (asf(yv[2][2 * k]) + asf(yv[3][2 * k]))));
        r[k] = (k ? bfhi(xx) : bflo(xx)) + rb(sv * gt[k]);
      }
      if (lane < 4) *(GAS u32*)(p.out + 8 * fi + 2 * e) = pk2(r[0], r[1]);
      if (fi == 0 && lane == 0) __hip_atomic_store((GAS u32*)(p.ws + WS_E), tag, __ATOMIC_RELAXED, __HIP_MEMORY_SCOPE_AGENT);
    }
  } else if (wave <= 3) {
    const int hw = wave - 1, tau = tid - 64;
    const int ch = tau & 7, rr = tau >> 3;
    const int kq = b & 3, row0 = kq * 144 + rr;
    const int cq = b >> 2;
    const u32 E = __hip_atomic_load((GAS u32*)(p.ws + WS_E), __ATOMIC_RELAXED, __HIP_MEMORY_SCOPE_AGENT);
    if (hw == 0 && wi == 0 && lane == 0) __hip_atomic_store((GAS u32*)(p.ws + WS_CNT + g * 256), 0u, __ATOMIC_RELAXED, __HIP_MEMORY_SCOPE_AGENT);
    asm volatile("" ::: "memory");
    u16 xs[9], xm[6];
#pragma unroll
    for (int i = 0; i < 9; ++i) xs[i] = p.x[lane + 64 * i];
#pragma unroll
    for (int i = 0; i < 6; ++i) xm[i] = p.x[row0 + 24 * i];
    u32x4 wa_[6], wb_[6];
#pragma unroll
    for (int i = 0; i < 6; ++i) wa_[i] = ldnt(p.wq + (size_t)(row0 + 24 * i) * NQ + cq * 64 + ch * 8);
    if (hw == 0) CTL[lane] = 0;
    __builtin_amdgcn_sched_barrier(0);
    wg_barrier();
    __builtin_amdgcn_sched_barrier(0);
    const u32 tag = __builtin_amdgcn_readfirstlane(E) + 1u;
    if (hw == 0 && lane == 0) CTL[C_TAG] = tag;
    float sum = 0.f;
#pragma unroll
    for (int i = 0; i < 9; ++i) sum += bflo(xs[i]);
    const float mean = wave_sum(sum) * (1.f / DM);
    float var = 0.f;
#pragma unroll
    for (int i = 0; i < 9; ++i) { const float d = bflo(xs[i]) - mean; var = fmaf(d, d, var); }
    var = wave_sum(var) * (1.f / DM);
    const float mb = rb(mean), den = rb(sqrtf(rb(rb(var) + 1e-5f)));
    float xn[6];
#pragma unroll
    for (int i = 0; i < 6; ++i) xn[i] = rb(rb(bflo(xm[i]) - mb) / den);
    LAS float* RS = (LAS float*)(L8 + L_RS);
    {
      float a[8] = {0.f, 0.f, 0.f, 0.f, 0.f, 0.f, 0.f, 0.f};
#pragma unroll
      for (int i = 0; i < 6; ++i) fma8(a, wa_[i], xn[i]);
#pragma unroll
      for (int k = 0; k < 8; ++k) a[k] = oct_sum(a[k]);
      if (lane < 8) { *(LAS f32x4*)(RS + hw * 64 + lane * 8) = f32x4{a[0], a[1], a[2], a[3]}; *(LAS f32x4*)(RS + hw * 64 + lane * 8 + 4) = f32x4{a[4], a[5], a[6], a[7]}; }
      vm_drain();
      if (lds_arrive(&CTL[C_CNTQ], lane) == 2 && lane < 32) {
        const f32x2 t = *(LAS f32x2*)(RS + lane * 2) + *(LAS f32x2*)(RS + 64 + lane * 2) + *(LAS f32x2*)(RS + 128 + lane * 2);
        g_st(rws, WS_Q + (u32)(kq * NQ + cq * 64 + lane * 2) * 8, asu(t[0]), asu(t[1]), tag);
      }
    }
#pragma unroll 1
    for (int pass = 0; pass < ((wi >> 1) == (g & 1) ? 2 : 1); ++pass) {
      const int cs = pass == 0 ? cq : 64 + (g >> 1);
#pragma unroll
      for (int i = 0; i < 6; ++i) { wa_[i] = ldnt(p.wg + (size_t)(row0 + 24 * i) * FF + cs * 64 + ch * 8); wb_[i] = ldnt(p.wu + (size_t)(row0 + 24 * i) * FF + cs * 64 + ch * 8); }
      float a[8] = {0.f, 0.f, 0.f, 0.f, 0.f, 0.f, 0.f, 0.f}, c[8] = {0.f, 0.f, 0.f, 0.f, 0.f, 0.f, 0.f, 0.f};
#pragma unroll
      for (int i = 0; i < 6; ++i) { fma8(a, wa_[i], xn[i]); fma8(c, wb_[i], xn[i]); }
#pragma unroll
      for (int k = 0; k < 8; ++k) { a[k] = oct_sum(a[k]); c[k] = oct_sum(c[k]); }
      LAS float* RG = RS + 192 + pass * 384; LAS float* RU = RG + 192;
      if (lane < 8) {
        *(LAS f32x4*)(RG + hw * 64 + lane * 8) = f32x4{a[0], a[1], a[2], a[3]}; *(LAS f32x4*)(RG + hw * 64 + lane * 8 + 4) = f32x4{a[4], a[5], a[6], a[7]};
        *(LAS f32x4*)(RU + hw * 64 + lane * 8) = f32x4{c[0], c[1], c[2], c[3]}; *(LAS f32x4*)(RU + hw * 64 + lane * 8 + 4) = f32x4{c[4], c[5], c[6], c[7]};
      }
      if (lds_arrive(&CTL[C_CNTF + pass], lane) == 2) {
        const LAS float* R = (lane < 32 ? RG : RU) + (lane & 31) * 2;
        const f32x2 t = *(const LAS f32x2*)R + *(const LAS f32x2*)(R + 64) + *(const LAS f32x2*)(R + 128);
        g_st(rws, (lane < 32 ? WS_G : WS_U) + (u32)(kq * FF + cs * 64 + (lane & 31) * 2) * 8, asu(t[0]), asu(t[1]), tag);
      }
    }
    const bool ptask = hw == 0 ? io < 192 : (hw == 1 && id < 216);
    const int rg = hw == 1 ? id % 72 : (io & 63), cg = hw == 1 ? id / 72 : io >> 6;
    u32x4 af[2];
    if (hw == 1 && ptask) {
      lds_spin<16>(&CTL[C_PUB], 48);
      u32x4 gu[8]; u32 of[8];
#pragma unroll
      for (int k = 0; k < 8; ++k) { of[k] = (k < 4 ? WS_G : WS_U) + (u32)((k & 3) * FF + 64 * rg + 2 * (lane & 31)) * 8; gu[k] = u32x4{0u, 0u, 0u, 0u}; }
      gather<8, 32>(rws, of, gu, tag);
      float hh[2];
#pragma unroll
      for (int k = 0; k < 2; ++k) {
        const float z = rb((asf(gu[0][2 * k]) + asf(gu[1][2 * k])) + (asf(gu[2][2 * k]) + asf(gu[3][2 * k])));
        const float u = rb((asf(gu[4][2 * k]) + asf(gu[5][2 * k])) + (asf(gu[6][2 * k]) + asf(gu[7][2 * k])));
        const float ge = rb(0.5f * z * (1.f + tanhf(0.7978845608028654f * (z + 0.044715f * z * z * z))));
        hh[k] = ge * u;
      }
      const u32 hp = pk2(hh[0], hh[1]);
#pragma unroll
      for (int ks = 0; ks < 2; ++ks)
#pragma unroll
        for (int i = 0; i < 4; ++i) af[ks][i] = __builtin_amdgcn_ds_bpermute((8 * i + 4 * ks + (lane >> 4)) * 4, hp);
    }
    lds_spin<4>(&CTL[C_QRDY], 1);
    for (int x = Q0 + hw;; x += NSCH) {
      int is; u32 sp = 0;
      while ((is = (int)CTL[C_ISS]) < 2 * x - LEAD && ++sp < SPIN_LDS / 8) { if (is == 0) __builtin_amdgcn_s_sleep(16); else __builtin_amdgcn_s_sleep(1); }
      if (is >= (int)ENDM || sp >= SPIN_LDS / 8) break;
      u32 t = 0xffffu;
      if (CTL[C_EMPTY] == 0) {
        if (lane == 0) t = __hip_atomic_fetch_add((GAS u32*)(p.ws + WS_CNT + g * 256), 1u, __ATOMIC_RELAXED, __HIP_MEMORY_SCOPE_AGENT);
        t = __builtin_amdgcn_readfirstlane(t);
        if (t > 0xffffu) t = 0xffffu;
      }
      CTL[C_TK + (x & 7)] = ((u32)(x + 1) << 16) | t;
      if (t >= (u32)NDT) { CTL[C_EMPTY] = 1; break; }
    }
    if (hw == 2 && io < 192) {
      const u32 off = WS_P + (u32)((rg >> 1) * 16 + (lane >> 3)) * P_STRIDE + (rg & 1) * 512 + (lane & 7) * 16;
      u32x4 d[9]; u32 of[9];
#pragma unroll
      for (int i = 0; i < 8; ++i) { of[i] = off + (i >> 2) * 8 * P_STRIDE + (i & 3) * 128; d[i] = u32x4{0u, 0u, 0u, 0u}; }
      of[8] = WS_ML + (u32)((rg >> 1) * 16 + (lane & 15)) * 16; d[8] = u32x4{0u, 0u, 0u, 0u};
      lds_spin<1>(&CTL[C_CNTM], NCONS);
      __builtin_amdgcn_s_sleep(20);
      gather<9, 0>(rws, of, d, tag);
      const float mj = asf(d[8][0]);
      const float wj = __builtin_amdgcn_exp2f(mj - row16_max(mj));
      const float sc = wj * __builtin_amdgcn_rcpf(row16_sum(wj * asf(d[8][2])));
      const float s0 = asf(__builtin_amdgcn_ds_bpermute((lane >> 3) * 4, asu(sc))), s1 = asf(__builtin_amdgcn_ds_bpermute((lane >> 3) * 4 + 32, asu(sc)));
      u32x4 o4;
#pragma unroll
      for (int i = 0; i < 4; ++i) o4[i] = pk2(oct_sum(fmaf(s0, asf(d[i][0]), s1 * asf(d[4 + i][0]))), oct_sum(fmaf(s0, asf(d[i][2]), s1 * asf(d[4 + i][2]))));
      if (lane < 8) *(volatile LAS u32x4*)(L8 + L_OS + lane * 16) = o4;
      CTL[C_ORDY] = 1;
    }
    if (ptask) {
      u32 lo = L_SC;
      if (hw == 1) {
        lds_spin<1>(&CTL[C_ISS], (int)ENDM);
        const int qa = ((int)(__builtin_amdgcn_readfirstlane(CTL[C_ISS]) - ENDM) + 1) & ~1;
        lds_spin<1>(&CTL[C_FREE + (qa & 7)], qa - 7); lds_spin<1>(&CTL[C_FREE + (qa & 7) + 1], qa - 6);
        lo = (u32)(qa & 7) * SLOTB;
      } else lds_spin<1>(&CTL[C_CNTV], NCONS);
      const int rs = hw == 0 ? 4 : 2, rbase = hw == 0 ? 128 * (rg >> 1) + 2 * (rg & 1) : 64 * rg;
      const u32x4 rw = mk_srd((hw == 0 ? p.wo : p.wd) + (size_t)rbase * DM + 192 * cg, ((31 * rs + 1) * DM + 192) * 2);
      u32 vo[3];
#pragma unroll
      for (int e = 0; e < 3; ++e) { const u32 u = (u32)(e * 64 + lane), row = u / 24u; vo[e] = ((row >> 1) * rs + (row & 1u)) * (DM * 2) + (u - row * 24u) * 16u; }
#pragma unroll 1
      for (int r = 0; r < 8; ++r)
#pragma unroll
        for (int e = 0; e < 3; ++e) dma1((u32)(u64)L8 + lo + r * 3072 + e * 1024, vo[e], rw, (u32)(4 * r * rs) * (DM * 2));
      vm_drain();
      LAS unsigned char* wa = L8 + lo + (16 * ((lane >> 3) & 1) + 2 * (lane >> 4) + ((lane >> 2) & 1)) * 384 + (lane & 3) * 8;
      bf16x8 wf[12][2];
#pragma unroll
      for (int t = 0; t < 12; ++t)
#pragma unroll
        for (int ks = 0; ks < 2; ++ks) wf[t][ks] = cat(tr_rd(wa + (8 * ks) * 384 + t * 32), tr_rd(wa + (8 * ks + 32) * 384 + t * 32));
      if (hw == 0) {
        lds_spin<0>(&CTL[C_ORDY], 1);
        af[0] = *(LAS u32x4*)(L8 + L_OS + (lane >> 4) * 16); af[1] = *(LAS u32x4*)(L8 + L_OS + 64 + (lane >> 4) * 16);
      }
      float y[12];
#pragma unroll
      for (int t = 0; t < 12; ++t) y[t] = mfma(__builtin_bit_cast(bf16x8, af[1]), wf[t][1], mfma(__builtin_bit_cast(bf16x8, af[0]), wf[t][0], f32x4{0.f, 0.f, 0.f, 0.f}))[0];
      const int G = lane >> 4;
      const bool odd = lane & 1, g0 = G & 1, g1 = G & 2;
      const float ya = sel(g1, sel(g0, y[6], y[4]), sel(g0, y[2], y[0])), yb = sel(g1, sel(g0, y[7], y[5]), sel(g0, y[3], y[1]));
      const float yc = sel(g0, y[10], y[8]), yd = sel(g0, y[11], y[9]);
      const float yan = dpp<XOR1>(ya), ybn = dpp<XOR1>(yb), ycn = dpp<XOR1>(yc), ydn = dpp<XOR1>(yd);
      const u32 base = (hw == 0 ? WS_Y : WS_F) + (u32)(rg * DM + 192 * cg + 32 * G + (odd ? 16 : 0) + (lane & 14)) * 8;
      g_st(rws, base, asu(odd ? ybn : ya), asu(odd ? yb : yan), tag);
      if (lane < 32) g_st(rws, base + 128 * 8, asu(odd ? ydn : yc), asu(odd ? yd : ycn), tag);
      if (hw == 1) CTL[C_FOUT] = 1;
    }
  } else {
    const int ci = wave - 4;
    const int n = lane & 15, G = lane >> 4;
    wg_barrier();
    asm volatile("" :: "v"(cpf0), "v"(cpf1));
    u32 ka[4], va[8];
#pragma unroll
    for (int i = 0; i < 4; ++i) ka[i] = n * 256 + (((4 * i + G) ^ n) << 4);
    bf16x8 kf[4][4];
    auto load_k = [&](int q) {
      lds_spin<1>(&CTL[C_PUB], 2 * q + 2);
      LAS unsigned char* sb = L8 + (q & (NSLOT - 1)) * SLOTB;
#pragma unroll
      for (int t = 0; t < 4; ++t)
#pragma unroll
        for (int i = 0; i < 4; ++i) kf[t][i] = *(LAS bf16x8*)(sb + ka[i] + t * 4096);
      asm volatile("s_waitcnt lgkmcnt(0)" : "+v"(kf[0][0]), "+v"(kf[0][1]), "+v"(kf[0][2]), "+v"(kf[0][3]), "+v"(kf[1][0]), "+v"(kf[1][1]), "+v"(kf[1][2]), "+v"(kf[1][3]),
                   "+v"(kf[2][0]), "+v"(kf[2][1]), "+v"(kf[2][2]), "+v"(kf[2][3]), "+v"(kf[3][0]), "+v"(kf[3][1]), "+v"(kf[3][2]), "+v"(kf[3][3]) :: "memory");
      CTL[C_FREE + (q & (NSLOT - 1))] = q + 1;
    };
    load_k(ci);
    lds_spin<2>(&CTL[C_CNTQ], 3);
    const u32 tag = CTL[C_TAG];
    if (ci == 0) {
      __builtin_amdgcn_s_sleep(8);
      u32x4 qv[8]; u32 of[8];
#pragma unroll
      for (int k = 0; k < 8; ++k) { of[k] = WS_Q + (u32)((k >> 1) * NQ + g * 256 + (k & 1) * 128 + 2 * lane) * 8; qv[k] = u32x4{0u, 0u, 0u, 0u}; }
      gather<8, 2>(rws, of, qv, tag);
#pragma unroll
      for (int j = 0; j < 2; ++j) {
        const float q0 = (asf(qv[j][0]) + asf(qv[2 + j][0])) + (asf(qv[4 + j][0]) + asf(qv[6 + j][0]));
        const float q1 = (asf(qv[j][2]) + asf(qv[2 + j][2])) + (asf(qv[4 + j][2]) + asf(qv[6 + j][2]));
        *(LAS u32*)(L8 + L_QS + j * 256 + lane * 4) = pk2(q0, q1);
      }
      lds_wait();
      CTL[C_QRDY] = 1;
    } else lds_spin<1>(&CTL[C_QRDY], 1);
    asm volatile("" ::: "memory");
    LAS unsigned char* SC = L8 + L_SC + ci * (NS / NCONS) * 256;
    float m, l;
    {
      bf16x8 qf[4][4];
#pragma unroll
      for (int i = 0; i < 4; ++i) {
        const u32x4 t4 = *(LAS u32x4*)(L8 + L_QS + (n & 1) * 256 + i * 64 + G * 16);
#pragma unroll
        for (int t = 0; t < 4; ++t) qf[t][i] = __builtin_bit_cast(bf16x8, (n >> 1) == t ? t4 : u32x4{0u, 0u, 0u, 0u});
      }
      const u32 sw = ((n >> 2) & 1) * 128 + (n & 1) * 64 + G * 16 + ((n >> 1) & 1) * 8;
      float mx = -INFINITY;
#pragma unroll 1
      for (int it = 0; it < NS / NCONS; ++it) {
        if (it > 0) load_k(it * NCONS + ci);
        f32x4 sa = {0.f, 0.f, 0.f, 0.f}, sb = {0.f, 0.f, 0.f, 0.f};
#pragma unroll
        for (int i = 0; i < 4; ++i) { sa = mfma(kf[0][i], qf[0][i], sa); sb = mfma(kf[2][i], qf[2][i], sb); sa = mfma(kf[1][i], qf[1][i], sa); sb = mfma(kf[3][i], qf[3][i], sb); }
        const f32x4 sc = sa + sb;
        const u32 c0 = score2(sc[0], sc[1]), c1 = score2(sc[2], sc[3]);
        mx = fmaxf(fmaxf(mx, fmaxf(bflo(c0), bfhi(c0))), fmaxf(bflo(c1), bfhi(c1)));
        if (n < 8) *(LAS u32x2*)(SC + it * 256 + sw) = u32x2{c0, c1};
      }
      mx = n < 8 ? mx : -INFINITY;
      mx = fmaxf(mx, dpp<XOR2>(mx)); mx = fmaxf(mx, dpp<ROR4>(mx)); mx = fmaxf(mx, dpp<ROR8>(mx)); mx = x32_max(x16_max(mx));
      m = ceilf(lg2u(mx));
      const float mo = dpp<XOR1>(m), mh = (((lane >> 2) ^ lane) & 1) ? mo : m;
      lds_wait();
      float ls = 0.f;
#pragma unroll
      for (int i = 0; i < (NS / NCONS + 3) / 4; ++i) {
        if (lane + 64 * i < NS / NCONS * 16) {
          LAS u32x4* cp = (LAS u32x4*)(SC + (lane + 64 * i) * 16);
          u32x4 z = *cp;
#pragma unroll
          for (int k = 0; k < 4; ++k) {
            const float p0 = __builtin_amdgcn_exp2f(lg2u(bflo(z[k])) - mh), p1 = __builtin_amdgcn_exp2f(lg2u(bfhi(z[k])) - mh);
            ls += p0 + p1; z[k] = pk2(p0, p1);
          }
          *cp = z;
        }
      }
      ls += dpp<XOR1>(ls); ls += dpp<XOR2>(ls); ls += dpp<ROR8>(ls); ls = x32_sum(x16_sum(ls));
      const float lo_ = dpp<ROR4>(ls);
      l = (((lane >> 2) ^ lane) & 1) ? lo_ : ls;
      lds_wait();
    }
    f32x4 acc[8];
    {
      const u32 row = 4 * G + (n >> 2), pp = n & 3;
#pragma unroll
      for (int u = 0; u < 8; ++u) { va[u] = row * 256 + ((((2 * u + (pp >> 1)) ^ ((row & 7) << 1))) << 4) + (pp & 1) * 8; acc[u] = f32x4{0.f, 0.f, 0.f, 0.f}; }
#pragma unroll 1
      for (int it = 0; it < NS / NCONS; ++it) {
        const int q = NS + it * NCONS + ci, s = q & (NSLOT - 1);
        const bf16x8 pf0 = *(LAS bf16x8*)(SC + it * 256 + (n & 1) * 64 + G * 16), pf1 = *(LAS bf16x8*)(SC + it * 256 + 128 + (n & 1) * 64 + G * 16);
        lds_spin<1>(&CTL[C_PUB], 2 * q + 2);
        LAS unsigned char* sb = L8 + s * SLOTB;
        s16x4 vf[2][8][2];
#pragma unroll
        for (int h = 0; h < 2; ++h)
#pragma unroll
          for (int u = 0; u < 8; ++u)
#pragma unroll
            for (int t = 0; t < 2; ++t) vf[h][u][t] = tr_rd(sb + va[u] + (2 * h + t) * 4096);
#pragma unroll
        for (int u = 0; u < 8; ++u) acc[u] = mfma(cat(vf[0][u][0], vf[0][u][1]), pf0, acc[u]);
        asm volatile("s_waitcnt lgkmcnt(0)" : "+v"(vf[1][0][0]), "+v"(vf[1][0][1]), "+v"(vf[1][1][0]), "+v"(vf[1][1][1]), "+v"(vf[1][2][0]), "+v"(vf[1][2][1]), "+v"(vf[1][3][0]), "+v"(vf[1][3][1]),
                     "+v"(vf[1][4][0]), "+v"(vf[1][4][1]), "+v"(vf[1][5][0]), "+v"(vf[1][5][1]), "+v"(vf[1][6][0]), "+v"(vf[1][6][1]), "+v"(vf[1][7][0]), "+v"(vf[1][7][1]) :: "memory");
        CTL[C_FREE + s] = q + 1;
#pragma unroll
        for (int u = 0; u < 8; ++u) acc[u] = mfma(cat(vf[1][u][0], vf[1][u][1]), pf1, acc[u]);
      }
    }
    lds_arrive(&CTL[C_CNTV], lane);
    {
      bf16x8 qf[4];
#pragma unroll
      for (int i = 0; i < 4; ++i) qf[i] = *(LAS bf16x8*)(L8 + L_QS + (n & 1) * 256 + i * 64 + G * 16);
      float ld = 0.f;
#pragma unroll 1
      for (int q = Q0 + ci;; q += NCONS) {
        const int s = q & (NSLOT - 1);
        bool go; u32 en;
        for (u32 sp = 0;;) {
          en = __builtin_amdgcn_readfirstlane(CTL[C_ISS]);
          go = (int)CTL[C_PUB] >= 2 * q + 1;
          if (go || (en >= ENDM && q >= (int)(en - ENDM)) || ++sp >= SPIN_LDS) break;
          __builtin_amdgcn_s_sleep(1);
        }
        if (!go) break;
        asm volatile("" ::: "memory");
        LAS unsigned char* sb = L8 + s * SLOTB;
        float z[8];
#pragma unroll
        for (int t = 0; t < 2; ++t) {
          f32x4 sc = {0.f, 0.f, 0.f, 0.f};
#pragma unroll
          for (int i = 0; i < 4; ++i) sc = mfma(*(LAS bf16x8*)(sb + ka[i] + t * 4096), qf[i], sc);
#pragma unroll
          for (int k = 0; k < 2; ++k) { const u32 c = score2(sc[2 * k], sc[2 * k + 1]); z[4 * t + 2 * k] = lg2u(bflo(c)); z[4 * t + 2 * k + 1] = lg2u(bfhi(c)); }
        }
        const float zm = fmaxf(fmaxf(fmaxf(z[0], z[1]), fmaxf(z[2], z[3])), fmaxf(fmaxf(z[4], z[5]), fmaxf(z[6], z[7])));
        if (__any(zm > m)) {
          const float mn = fmaxf(m, ceilf(x32_max(x16_max(zm))));
          const float al = __builtin_amdgcn_exp2f(m - mn);
          l *= al; ld *= al; m = mn;
#pragma unroll
          for (int u = 0; u < 8; ++u) acc[u] *= al;
        }
        u32x4 pw;
#pragma unroll
        for (int k = 0; k < 4; ++k) { const float p0 = __builtin_amdgcn_exp2f(z[2 * k] - m), p1 = __builtin_amdgcn_exp2f(z[2 * k + 1] - m); ld += p0 + p1; pw[k] = pk2(p0, p1); }
        asm volatile("" : "+v"(pw));
        en = __builtin_amdgcn_readfirstlane(CTL[C_ISS]);
        lds_spin<0>(&CTL[C_PUB], 2 * q + 2);
#pragma unroll
        for (int u = 0; u < 8; ++u) acc[u] = mfma(cat(tr_rd(sb + va[u] + 8192), tr_rd(sb + va[u] + 12288)), __builtin_bit_cast(bf16x8, pw), acc[u]);
        lds_wait();
        CTL[C_FREE + s] = q + 1;
        if (en >= ENDM && q + NCONS >= (int)(en - ENDM)) break;
      }
      l += x32_sum(x16_sum(ld));
    }
    {
      const bool u0 = n & 2, u1 = n & 4, u2 = n & 8;
      f32x4 o;
#pragma unroll
      for (int k = 0; k < 4; ++k) {
        const float b0 = sel(u0, acc[1][k], acc[0][k]), b1 = sel(u0, acc[3][k], acc[2][k]), b2 = sel(u0, acc[5][k], acc[4][k]), b3 = sel(u0, acc[7][k], acc[6][k]);
        o[k] = sel(u2, sel(u1, b3, b2), sel(u1, b1, b0));
      }
      LAS unsigned char* MR = L8 + L_MRG;
      *(volatile LAS f32x4*)(MR + ci * 1024 + lane * 16) = o;
      if (lane < 2) *(volatile LAS f32x2*)(MR + 4096 + ci * 16 + lane * 8) = f32x2{m, l};
      u32 old = 0;
      if (lane == 0) asm volatile("ds_add_rtn_u32 %0, %1, %2" : "=v"(old) : "v"((u32)(u64)&CTL[C_CNTM]), "v"(1u) : "memory");
      f32x4 oc[4]; f32x2 mc[4];
#pragma unroll
      for (int c = 0; c < 4; ++c) { oc[c] = *(volatile LAS f32x4*)(MR + c * 1024 + lane * 16); mc[c] = *(volatile LAS f32x2*)(MR + 4096 + c * 16 + (lane & 1) * 8); }
      asm volatile("s_waitcnt lgkmcnt(0)" : "+v"(old) :: "memory");
      if (__builtin_amdgcn_readfirstlane(old) == NCONS - 1) {
        const float M = fmaxf(fmaxf(mc[0][0], mc[1][0]), fmaxf(mc[2][0], mc[3][0]));
        f32x4 t = {0.f, 0.f, 0.f, 0.f}; float L = 0.f;
#pragma unroll
        for (int c = 0; c < 4; ++c) { const float w = __builtin_amdgcn_exp2f(mc[c][0] - M); t += w * oc[c]; L = fmaf(w, mc[c][1], L); }
        const u32 base = WS_P + (u32)((2 * g + (n & 1)) * 16 + wi) * P_STRIDE + ((n >> 1) * 4 + G) * 16;
        g_st(rws, base, asu(t[0]), asu(t[1]), tag);
        g_st(rws, base + 512, asu(t[2]), asu(t[3]), tag);
        if (lane < 2) g_st(rws, WS_ML + (u32)((2 * g + lane) * 16 + wi) * 16, asu(M), asu(L), tag);
      }
    }
  }
}
}

extern "C" void kernel_launch(void* const* d_in, const int* in_sizes, int n_in, void* d_out, int out_size, void* d_ws, size_t ws_size, hipStream_t stream) {
  fk::Params p;
  p.x = (const u16*)d_in[0]; p.k = (const u16*)d_in[1]; p.v = (const u16*)d_in[2]; p.wq = (const u16*)d_in[3]; p.wo = (const u16*)d_in[4];
  p.wg = (const u16*)d_in[6]; p.wu = (const u16*)d_in[7]; p.wd = (const u16*)d_in[8];
  p.out = (u16*)d_out; p.ws = (unsigned char*)d_ws;
  hipLaunchKernelGGL(fk::fused_decode, dim3(fk::NWG), dim3(fk::NTHR), 0, stream, p);
}
```
